```python
import torch

def _softmax(x, axis=-1):
    return torch.softmax(x, dim=axis)
def _gelu(x):
    return torch.nn.functional.gelu(x, approximate="tanh")
def _silu(x):
    return torch.nn.functional.silu(x)
def _sigmoid(x):
    return torch.sigmoid(x)
def _layernorm(x, axis=-1, eps=1e-5):
    m = x.mean(dim=axis, keepdim=True)
    v = x.var(dim=axis, keepdim=True, unbiased=False)
    return (x - m) / torch.sqrt(v + eps)
def _scatter_add(tgt, idx, src, axis=0):
    return tgt.clone().index_add_(axis, idx, src)
def _segment_sum(src, seg, n):
    out = torch.zeros((n,) + src.shape[1:], dtype=src.dtype)
    return out.index_add_(0, seg, src)
def _pad(x, pw):
    flat = tuple(v for pair in reversed(pw) for v in pair)
    return torch.nn.functional.pad(x, flat)

kernel_name = 'sequential_preLN__softmax_full_mha__mixer_channel_mlp__prompt'

def setup_inputs():

    _w = (lambda a: (lambda t: t.to(torch.bfloat16) if t.is_floating_point() else t)(torch.as_tensor(a)))
    def _A_setup():
        B, S, D = 1, 2048, 448
        return {
            "x": _w((lambda *s: torch.randn(*s, dtype=torch.bfloat16) * 0.05)(B, S, D)),
            "wqkv": _w((lambda *s: torch.randn(*s, dtype=torch.bfloat16) * 0.05)(D, 3 * D)),
            "wo": _w((lambda *s: torch.randn(*s, dtype=torch.bfloat16) * 0.05)(D, D)),
        }
    _A_in = _A_setup()

    def _B_setup():
        B, S, D, F = 1, 2048, 448, 1792
        return {
            "x": _w((lambda *s: torch.randn(*s, dtype=torch.bfloat16) * 0.05)(B, S, D)),
            "w1": _w((lambda *s: torch.randn(*s, dtype=torch.bfloat16) * 1.6)(D, F) * (D ** -0.5)),
            "b1": _w((lambda *s: torch.randn(*s, dtype=torch.bfloat16) * 0.8)(F)),
            "w2": _w((lambda *s: torch.randn(*s, dtype=torch.bfloat16) * 0.05)(F, D) * (F ** -0.5)),
            "b2": _w((lambda *s: torch.randn(*s, dtype=torch.bfloat16) * 0.05)(D)),
        }
    _B_in = _B_setup()

    return {
        "A_x": _A_in["x"],
        "A_wqkv": _A_in["wqkv"],
        "A_wo": _A_in["wo"],
        "B_x": _B_in["x"],
        "B_w1": _B_in["w1"],
        "B_b1": _B_in["b1"],
        "B_w2": _B_in["w2"],
        "B_b2": _B_in["b2"],
    }

def reference(A_x, A_wqkv, A_wo, B_x, B_w1, B_b1, B_w2, B_b2):
    def _A_ref(x, wqkv, wo):
        B, S, D = x.shape
        H = 16; Dh = D // H
        qkv = (lambda x, *d: x.permute(*d))((x @ wqkv).reshape(B, S, 3, H, Dh), 2, 0, 3, 1, 4)
        q, k, v = qkv[0], qkv[1], qkv[2]
        scale = Dh ** -0.5
        scores = torch.einsum("bhqd,bhkd->bhqk", q, k) * scale
        p = _softmax(scores, axis=-1)
        o = torch.einsum("bhqk,bhkd->bhqd", p, v)
        return (lambda x, *d: x.permute(*d))(o, 0, 2, 1, 3).reshape(B, S, D) @ wo

    def _B_ref(x, w1, b1, w2, b2):
        y = _layernorm(x, axis=-1)
        h = _gelu(torch.einsum("bsd,df->bsf", y, w1) + b1)
        return torch.einsum("bsf,fd->bsd", h, w2) + b2

    x = A_x
    h = x + _A_ref(_layernorm(x, axis=-1), A_wqkv, A_wo)
    h2 = h + _B_ref(_layernorm(h, axis=-1), B_w1, B_b1, B_w2, B_b2)
    return h2
```

```cpp
#include <hip/hip_runtime.h>
#include <hip/hip_bf16.h>
#include <stdint.h>

typedef unsigned short u16;
static constexpr int S = 2048, D = 448, H = 16, DH = 28, F = 1792;

__device__ __forceinline__ float bf2f(u16 v) { return __uint_as_float(((unsigned)v) << 16); }
__device__ __forceinline__ u16 f2bf(float f) { __hip_bfloat16 b = __float2bfloat16(f); return *(u16*)&b; }

__global__ void ln_kernel(const u16* __restrict__ x, u16* __restrict__ y, int twice) {
    int row = blockIdx.x * 4 + (threadIdx.x >> 6), lane = threadIdx.x & 63;
    float v[7];
    for (int i = 0; i < 7; ++i) v[i] = bf2f(x[row * D + i * 64 + lane]);
    for (int t = 0; t <= twice; ++t) {
        float s = 0; for (int i = 0; i < 7; ++i) s += v[i];
        for (int o = 32; o; o >>= 1) s += __shfl_xor(s, o);
        float m = s / D; float q = 0; for (int i = 0; i < 7; ++i) { v[i] -= m; q += v[i] * v[i]; }
        for (int o = 32; o; o >>= 1) q += __shfl_xor(q, o);
        float r = rsqrtf(q / D + 1e-5f);
        for (int i = 0; i < 7; ++i) v[i] *= r;
    }
    for (int i = 0; i < 7; ++i) y[row * D + i * 64 + lane] = f2bf(v[i]);
}

__global__ void gemm_kernel(const u16* __restrict__ A, const u16* __restrict__ B, u16* __restrict__ C,
                            const u16* __restrict__ bias, const u16* __restrict__ res, int M, int N, int K, int act) {
    __shared__ float As[16][17], Bs[16][17];
    int tx = threadIdx.x & 15, ty = threadIdx.x >> 4;
    int row = blockIdx.y * 16 + ty, col = blockIdx.x * 16 + tx;
    float acc = 0;
    for (int k0 = 0; k0 < K; k0 += 16) {
        As[ty][tx] = bf2f(A[row * K + k0 + tx]);
        Bs[ty][tx] = bf2f(B[(k0 + ty) * N + col]);
        __syncthreads();
        for (int k = 0; k < 16; ++k) acc += As[ty][k] * Bs[k][tx];
        __syncthreads();
    }
    if (bias) acc += bf2f(bias[col]);
    if (act) { float u = 0.7978845608028654f * (acc + 0.044715f * acc * acc * acc); acc = 0.5f * acc * (1.f + tanhf(u)); }
    if (res) acc += bf2f(res[row * N + col]);
    C[row * N + col] = f2bf(acc);
}

__global__ void attn_kernel(const u16* __restrict__ qkv, u16* __restrict__ o) {
    int h = blockIdx.y, q = blockIdx.x * 64 + threadIdx.x;
    __shared__ float Ks[64][DH], Vs[64][DH];
    float qv[DH], acc[DH];
    for (int d = 0; d < DH; ++d) { qv[d] = bf2f(qkv[q * 3 * D + h * DH + d]) * 0.18898223650461361f; acc[d] = 0; }
    float m = -1e30f, l = 0;
    for (int k0 = 0; k0 < S; k0 += 64) {
        __syncthreads();
        for (int i = threadIdx.x; i < 64 * DH; i += 64) { int r = i / DH, c = i % DH;
            Ks[r][c] = bf2f(qkv[(k0 + r) * 3 * D + D + h * DH + c]); Vs[r][c] = bf2f(qkv[(k0 + r) * 3 * D + 2 * D + h * DH + c]); }
        __syncthreads();
        for (int k = 0; k < 64; ++k) {
            float s = 0;
#pragma unroll
            for (int d = 0; d < DH; ++d) s += qv[d] * Ks[k][d];
            float mn = fmaxf(m, s); float a = __expf(m - mn), p = __expf(s - mn);
            l = l * a + p;
#pragma unroll
            for (int d = 0; d < DH; ++d) acc[d] = acc[d] * a + p * Vs[k][d];
            m = mn;
        }
    }
    for (int d = 0; d < DH; ++d) o[q * D + h * DH + d] = f2bf(acc[d] / l);
}

extern "C" void kernel_launch(void* const* d_in, const int* in_sizes, int n_in,
                              void* d_out, int out_size, void* d_ws, size_t ws_size,
                              hipStream_t stream) {
    const u16* x = (const u16*)d_in[0]; const u16* wqkv = (const u16*)d_in[1]; const u16* wo = (const u16*)d_in[2];
    const u16* w1 = (const u16*)d_in[4]; const u16* b1 = (const u16*)d_in[5]; const u16* w2 = (const u16*)d_in[6]; const u16* b2 = (const u16*)d_in[7];
    u16* out = (u16*)d_out; u16* ws = (u16*)d_ws;
    u16* xn = ws; u16* qkv = xn + S * D; u16* o = qkv + S * 3 * D; u16* h = o + S * D; u16* y = h + S * D; u16* hid = y + S * D;
    ln_kernel<<<S / 4, 256, 0, stream>>>(x, xn, 0);
    gemm_kernel<<<dim3(3 * D / 16, S / 16), 256, 0, stream>>>(xn, wqkv, qkv, nullptr, nullptr, S, 3 * D, D, 0);
    attn_kernel<<<dim3(S / 64, H), 64, 0, stream>>>(qkv, o);
    gemm_kernel<<<dim3(D / 16, S / 16), 256, 0, stream>>>(o, wo, h, nullptr, x, S, D, D, 0);
    ln_kernel<<<S / 4, 256, 0, stream>>>(h, y, 1);
    gemm_kernel<<<dim3(F / 16, S / 16), 256, 0, stream>>>(y, w1, hid, b1, nullptr, S, F, D, 1);
    gemm_kernel<<<dim3(D / 16, S / 16), 256, 0, stream>>>(hid, w2, out, b2, h, S, D, F, 0);
}
```

```cpp
#include <hip/hip_runtime.h>
#include <stdint.h>

typedef unsigned short u16;
typedef __bf16 bf16x2 __attribute__((ext_vector_type(2)));
typedef __bf16 bf16x8 __attribute__((ext_vector_type(8)));
typedef __bf16 bf16x16 __attribute__((ext_vector_type(16)));
typedef float f32x2 __attribute__((ext_vector_type(2)));
typedef float f32x4 __attribute__((ext_vector_type(4)));
typedef float f32x8 __attribute__((ext_vector_type(8)));
typedef float f32x16 __attribute__((ext_vector_type(16)));
typedef unsigned u32x2 __attribute__((ext_vector_type(2)));
typedef unsigned u32x4 __attribute__((ext_vector_type(4)));
typedef short s16x4 __attribute__((ext_vector_type(4)));
#define LAS __attribute__((address_space(3)))
#define DI __device__ __forceinline__
#define SBAR() __builtin_amdgcn_sched_barrier(0)

constexpr int S_ = 2048, D_ = 448, F_ = 1792;
constexpr float EPS = 1e-5f;
constexpr size_t WS_Q = 0;
constexpr size_t WS_KV = WS_Q + 16 * 64 * 2048;
constexpr size_t WS_O = WS_KV + 16 * 64 * 4096;
constexpr size_t WS_H = WS_O + 64 * 32 * 1024;
constexpr size_t WS_ST = WS_H + 64 * 28 * 1024;
constexpr size_t WS_HID = WS_ST + 64 * 14 * 32 * 8;
constexpr size_t WS_W1 = WS_HID + 64 * 112 * 1024;
constexpr size_t WS_W2 = WS_W1 + 56 * 28 * 1024;
constexpr size_t WS_WO = WS_W2 + 14 * 112 * 1024;
constexpr size_t WS_END = WS_WO + 14 * 32 * 1024;

#define STAMP(k, i)

DI f32x16 mfma(u32x4 a, u32x4 b, f32x16 c) {
    return __builtin_amdgcn_mfma_f32_32x32x16_bf16(__builtin_bit_cast(bf16x8, a), __builtin_bit_cast(bf16x8, b), c, 0, 0, 0);
}
DI unsigned pk2(float lo, float hi) { f32x2 v = {lo, hi}; return __builtin_bit_cast(unsigned, __builtin_convertvector(v, bf16x2)); }
DI float bflo(unsigned u) { return __uint_as_float(u << 16); }
DI float bfhi(unsigned u) { return __uint_as_float(u & 0xffff0000u); }
DI float dot2(unsigned a, unsigned b, float c) { return __builtin_amdgcn_fdot2_f32_bf16(__builtin_bit_cast(bf16x2, a), __builtin_bit_cast(bf16x2, b), c, false); }
DI float swap32(float v) {
    auto r = __builtin_amdgcn_permlane32_swap(__float_as_uint(v), __float_as_uint(v), false, false);
    return __uint_as_float((threadIdx.x & 32) ? r[0] : r[1]);
}
DI float sum32(float v) { auto r = __builtin_amdgcn_permlane32_swap(__float_as_uint(v), __float_as_uint(v), false, false); return __uint_as_float(r[0]) + __uint_as_float(r[1]); }
DI float max32(float v) { auto r = __builtin_amdgcn_permlane32_swap(__float_as_uint(v), __float_as_uint(v), false, false); return fmaxf(__uint_as_float(r[0]), __uint_as_float(r[1])); }
DI void glds16(const void* g, unsigned lds_base) {
    unsigned sv;
    asm volatile("s_mov_b32 %0, m0\n\ts_mov_b32 m0, %2\n\ts_nop 0\n\tglobal_load_lds_dwordx4 %1, off\n\ts_mov_b32 m0, %0" : "=&s"(sv) : "v"(g), "s"(lds_base) : "memory");
}
#define WAITV(N) asm volatile("s_waitcnt vmcnt(" #N ")" ::: "memory")
#define BARRIER() asm volatile("s_waitcnt lgkmcnt(0)\n\ts_barrier" ::: "memory")
DI u32x4 lds128(const LAS char* p) { return *(const LAS u32x4*)p; }
DI u32x2 ldstr(const LAS char* p) { return __builtin_bit_cast(u32x2, __builtin_amdgcn_ds_read_tr16_b64_v4i16((LAS s16x4*)p)); }
DI void split2(float v, unsigned& hi, unsigned& lo) { hi = pk2(v, 0.f) & 0xffffu; lo = pk2(v - bflo(hi), 0.f) & 0xffffu; }
DI u32x4 pack8(const f32x16& a, int s) {
    u32x4 r;
    r[0] = pk2(a[8 * s + 0], a[8 * s + 1]); r[1] = pk2(a[8 * s + 2], a[8 * s + 3]);
    r[2] = pk2(a[8 * s + 4], a[8 * s + 5]); r[3] = pk2(a[8 * s + 6], a[8 * s + 7]);
    return r;
}

constexpr int XCH = 16384, WCH = 12288, CHB = XCH + WCH, K1_MISC = 5 * CHB, K1_LDS = K1_MISC + 4096;

template <int NV>
DI void qkv_compute(LAS char* lds, char* ws, int w, int lane, int rb, int ct) {
    const int i = lane & 31, h = lane >> 5;
    const int row = 32 * w + i;
    const int xoff = row * 128, xsw = (row >> 1) & 7;
    const int woff = XCH + (8 * h + ((lane & 15) >> 2)) * 192 + ((168 * ct) & 15) + (((lane >> 4) & 1) * 16 + (lane & 3) * 4) * 2;
    f32x16 acc[3] = {{}, {}, {}};
    float sx = 0.f, sq = 0.f, cs[3] = {0.f, 0.f, 0.f};
    const unsigned ONES = 0x3f803f80u;
#pragma unroll
    for (int c = 0; c < 7; ++c) {
        BARRIER();
        LAS char* base = lds + (c % 5) * CHB;
#pragma unroll
        for (int ks = 0; ks < 4; ++ks) {
            u32x4 xf = lds128(base + xoff + (((2 * ks + h) ^ xsw) << 4));
            u32x4 wf[3];
#pragma unroll
            for (int s = 0; s < 3; ++s) {
                u32x2 a = ldstr(base + woff + ks * 16 * 192 + s * 56), b = ldstr(base + woff + (ks * 16 + 4) * 192 + s * 56);
                wf[s] = (u32x4){a[0], a[1], b[0], b[1]};
            }
#pragma unroll
            for (int s = 0; s < 3; ++s) acc[s] = (s >= 3 - NV) ? mfma(xf, wf[s], acc[s]) : mfma(wf[s], xf, acc[s]);
#pragma unroll
            for (int e = 0; e < 4; ++e) { sx = dot2(xf[e], ONES, sx); sq = dot2(xf[e], xf[e], sq); }
            if (ks == w) {
#pragma unroll
                for (int s = 0; s < 3; ++s)
#pragma unroll
                    for (int e = 0; e < 4; ++e) cs[s] = dot2(wf[s][e], ONES, cs[s]);
            }
        }
    }
    sx = sum32(sx); sq = sum32(sq);
    const float mean = sx * (1.f / D_);
    const float var = fmaxf(sq * (1.f / D_) - mean * mean, 0.f);
    const float rstd = __builtin_amdgcn_rsqf(var + EPS);
    LAS float* csb = (LAS float*)(lds + K1_MISC);
    LAS float* rsb = (LAS float*)(lds + K1_MISC + 2048) + w * 32;
#pragma unroll
    for (int s = 0; s < 3; ++s) { float t = sum32(cs[s]); if (h == 0) csb[(w * 3 + s) * 32 + i] = t; }
    if (h == 0) rsb[i] = rstd;
    BARRIER();
    unsigned mh, ml; split2(-mean, mh, ml);
    u32x4 xe = {h ? 0u : (mh | (mh << 16)), h ? 0u : (ml | (ml << 16)), 0u, 0u};
#pragma unroll
    for (int s = 0; s < 3; ++s) {
        float t = csb[(0 * 3 + s) * 32 + i] + csb[(1 * 3 + s) * 32 + i] + csb[(2 * 3 + s) * 32 + i] + csb[(3 * 3 + s) * 32 + i];
        unsigned ch, cl; split2(t, ch, cl);
        const unsigned p = h ? 0u : (ch | (cl << 16));
        u32x4 we = {p, p, 0u, 0u};
        acc[s] = (s >= 3 - NV) ? mfma(xe, we, acc[s]) : mfma(we, xe, acc[s]);
    }
    const int tile = rb * 4 + w;
#pragma unroll
    for (int s = 0; s < 3; ++s) {
        const int sec = 3 * ct + s, head = sec & 15;
        if (s >= 3 - NV) {
            f32x16 v = acc[s];
#pragma unroll
            for (int g = 0; g < 4; ++g) {
                f32x4 r4 = *(const LAS f32x4*)(rsb + 8 * g + 4 * h);
#pragma unroll
                for (int e = 0; e < 4; ++e) v[4 * g + e] = i < 28 ? v[4 * g + e] * r4[e] : (i == 28 ? 1.f : 0.f);
            }
            char* dst = ws + WS_KV + (size_t)((head * 64 + tile) * 4 + 2) * 1024 + lane * 16;
            *(u32x4*)dst = pack8(v, 0); *(u32x4*)(dst + 1024) = pack8(v, 1);
        } else {
            const bool isq = sec < 16;
            const float sc = isq ? rstd * (0.18898223650461361f * 1.4426950408889634f) : rstd;
            f32x16 v = acc[s] * sc;
            u32x4 p0 = pack8(v, 0), p1 = pack8(v, 1);
            if (h) { p1[2] = isq ? 0u : 0x3f803f80u; p1[3] = isq ? 0u : 0x00003f80u; }
            const int ip = (i & 19) | ((i & 4) << 1) | ((i & 8) >> 1);
            char* dst = isq ? ws + WS_Q + (size_t)((head * 64 + tile) * 2) * 1024 + lane * 16
                            : ws + WS_KV + (size_t)((head * 64 + tile) * 4) * 1024 + (h * 32 + ip) * 16;
            *(u32x4*)dst = p0; *(u32x4*)(dst + 1024) = p1;
        }
    }
}

struct RetileUnit { const u16* src; int ld; char* dst; int tstride; };
DI void retile_load(int u, int lane, const u16* wo, const u16* w1, const u16* w2, unsigned (&v)[8]) {
    const int c = lane & 31, h = lane >> 5;
    if (u < 784) {
        const int win = u / 28, ks = u % 28;
#pragma unroll
        for (int j = 0; j < 8; ++j) v[j] = *(const unsigned*)(w1 + (size_t)(16 * ks + 8 * (j >> 2) + 4 * h + (j & 3)) * F_ + win * 64 + 2 * c);
    } else if (u < 1568) {
        const int q = u - 784, win = q / 112, ks = q % 112;
#pragma unroll
        for (int j = 0; j < 8; ++j) v[j] = *(const unsigned*)(w2 + (size_t)(16 * ks + 8 * (j >> 2) + 4 * h + (j & 3)) * D_ + win * 64 + 2 * c);
    } else {
        const int q = u - 1568, win = q / 32, ks = q % 32;
#pragma unroll
        for (int j = 0; j < 8; ++j) {
            const int d = 16 * (ks & 1) + 8 * (j >> 2) + 4 * h + (j & 3);
            const unsigned t = *(const unsigned*)(wo + (size_t)((ks >> 1) * 28 + (d < 28 ? d : 0)) * D_ + win * 64 + 2 * c);
            v[j] = d < 28 ? t : 0u;
        }
    }
}
DI void retile_store(int u, int lane, char* ws, const unsigned (&v)[8]) {
    const int c = lane & 31, h = lane >> 5;
    char* dst;
    if (u < 784) { const int win = u / 28, ks = u % 28; dst = ws + WS_W1 + (size_t)((2 * win + (c >> 4)) * 28 + ks) * 1024; }
    else if (u < 1568) { const int q = u - 784, win = q / 112, ks = q % 112; dst = ws + WS_W2 + (size_t)((2 * win + (c >> 4)) * 112 + ks) * 1024; }
    else { const int q = u - 1568, win = q / 32, ks = q % 32; dst = ws + WS_WO + (size_t)((2 * win + (c >> 4)) * 32 + ks) * 1024; }
    dst += (h * 32 + ((2 * c) & 31)) * 16;
    u32x4 e, o;
#pragma unroll
    for (int j = 0; j < 4; ++j) { e[j] = __builtin_amdgcn_perm(v[2 * j + 1], v[2 * j], 0x05040100u); o[j] = __builtin_amdgcn_perm(v[2 * j + 1], v[2 * j], 0x07060302u); }
    *(u32x4*)dst = e; *(u32x4*)(dst + 16) = o;
}

__global__ __launch_bounds__(512, 2) void k1_qkv(const u16* __restrict__ x, const u16* __restrict__ wqkv, const u16* __restrict__ wo,
                                                  const u16* __restrict__ w1, const u16* __restrict__ w2, char* __restrict__ ws) {
    extern __shared__ __attribute__((aligned(16))) char smem[];
    LAS char* lds = (LAS char*)smem;
    const int lane = threadIdx.x & 63, w = __builtin_amdgcn_readfirstlane(threadIdx.x >> 6);
    const int b = blockIdx.x, xcd = b & 7, jj = b >> 3;
    const int rb = 4 * (xcd >> 1) + (jj & 3), ct = 8 * (xcd & 1) + (jj >> 2);
    STAMP(0, 0);
    if (w >= 4) {
        const int hw = w - 4;
        const unsigned lbase = (unsigned)(uintptr_t)lds;
        const char* xs[4]; const char* wsrc[3];
#pragma unroll
        for (int p = 0; p < 4; ++p) { const int row = 8 * (4 * hw + p) + (lane >> 3); xs[p] = (const char*)x + (size_t)(rb * 128 + row) * 896 + (((lane & 7) ^ ((row >> 1) & 7)) << 4); }
        int wo_[3];
#pragma unroll
        for (int p = 0; p < 3; ++p) { const int q = (3 * hw + p) * 64 + lane, row = q / 12, pc = q % 12; wo_[p] = row * 2688 + ((168 * ct) & ~15) + pc * 16; }
        auto issue = [&](int c) {
            const unsigned sb = lbase + (c % 5) * CHB;
#pragma unroll
            for (int p = 0; p < 4; ++p) glds16(xs[p] + c * 128, sb + (4 * hw + p) * 1024);
#pragma unroll
            for (int p = 0; p < 3; ++p) { int o = wo_[p] + c * 64 * 2688; o = min(o, 448 * 2688 - 16); glds16((const char*)wqkv + o, sb + XCH + (3 * hw + p) * 1024); }
        };
        issue(0); issue(1); issue(2); issue(3); issue(4);
        WAITV(28); BARRIER();
        WAITV(21); BARRIER(); issue(5);
        WAITV(21); BARRIER(); issue(6);
        WAITV(21); BARRIER();
        WAITV(14); BARRIER();
        WAITV(7); BARRIER();
        WAITV(0); BARRIER();
        return;
    }
    const int u0 = b * 4 + w, u1 = u0 + 1024;
    unsigned rv0[8], rv1[8];
    retile_load(u0, lane, wo, w1, w2, rv0);
    if (u1 < 1792) retile_load(u1, lane, wo, w1, w2, rv1);
    if (ct < 10) qkv_compute<0>(lds, ws, w, lane, rb, ct);
    else if (ct == 10) qkv_compute<1>(lds, ws, w, lane, rb, ct);
    else qkv_compute<3>(lds, ws, w, lane, rb, ct);
    retile_store(u0, lane, ws, rv0);
    if (u1 < 1792) retile_store(u1, lane, ws, rv1);
    STAMP(0, 1);
}

constexpr int K2_RED = 131072, K2_LDS = K2_RED + 4 * 4096 + 64;
DI f32x16 pv(u32x4 v, const f32x16& p, int s, f32x16 o) {
    f32x8 ph = {p[8 * s], p[8 * s + 1], p[8 * s + 2], p[8 * s + 3], p[8 * s + 4], p[8 * s + 5], p[8 * s + 6], p[8 * s + 7]};
    return __builtin_amdgcn_smfmac_f32_32x32x32_bf16(__builtin_bit_cast(bf16x8, v), __builtin_bit_cast(bf16x16, ph), o, 0xDDDD, 0, 0);
}
#define EXP4(P, i) do { P[i] = __builtin_amdgcn_exp2f(P[i]); P[i + 1] = __builtin_amdgcn_exp2f(P[i + 1]); P[i + 2] = __builtin_amdgcn_exp2f(P[i + 2]); P[i + 3] = __builtin_amdgcn_exp2f(P[i + 3]); } while (0)
#define PIN(x) asm volatile("" : "+v"(x))

__global__ __launch_bounds__(512, 2) void k2_attn(char* __restrict__ ws) {
    extern __shared__ __attribute__((aligned(16))) char smem[];
    LAS char* lds = (LAS char*)smem;
    const int lane = threadIdx.x & 63, w = __builtin_amdgcn_readfirstlane(threadIdx.x >> 6);
    const int b = blockIdx.x, head = 2 * (b & 7) + ((b >> 3) & 1), rb = b >> 4;
    const int qt = w & 3, kh = w >> 2, h = lane >> 5;
    STAMP(1, 0);
    const char* kv = ws + WS_KV + (size_t)head * 64 * 4096;
    const char* src = kv + (size_t)(kh * 32 + qt) * 4096 + lane * 16;
    const unsigned ldst = (unsigned)(uintptr_t)lds + kh * 16384 + qt * 4096;
    auto issue = [&](int st) {
#pragma unroll
        for (int p = 0; p < 4; ++p) glds16(src + (size_t)st * 16384 + p * 1024, ldst + (st & 3) * 32768 + p * 1024);
    };
    const char* qsrc = ws + WS_Q + (size_t)((head * 64 + rb * 4 + qt) * 2) * 1024 + lane * 16;
    u32x4 q0 = *(const u32x4*)qsrc, q1 = *(const u32x4*)(qsrc + 1024);
    const f32x16 zero = {};
    f32x16 o;
    LAS char* rd = lds + kh * 16384 + lane * 16;
    for (int pass = 0; pass < 2; ++pass) {
        issue(0); issue(1); issue(2);
        float mref;
        STAMP(1, 2);
        WAITV(8); BARRIER();
        STAMP(1, 3);
        if (h) { q1[2] = 0u; q1[3] = 0u; }
        if (pass == 0) {
            f32x16 s = mfma(lds128(lds + lane * 16), q0, zero); s = mfma(lds128(lds + 1024 + lane * 16), q1, s);
            float m = s[0];
#pragma unroll
            for (int r = 1; r < 16; ++r) m = fmaxf(m, s[r]);
            mref = max32(m);
        } else {
            float m = -INFINITY;
            for (int t = 0; t < 64; ++t) {
                f32x16 s = mfma(*(const u32x4*)(kv + t * 4096 + lane * 16), q0, zero); s = mfma(*(const u32x4*)(kv + t * 4096 + 1024 + lane * 16), q1, s);
#pragma unroll
                for (int r = 0; r < 16; ++r) m = fmaxf(m, s[r]);
            }
            mref = max32(m);
        }
        {
            const float n = -mref; const unsigned a = pk2(n, 0.f) & 0xffffu; const float r1 = n - bflo(a);
            const unsigned bb = pk2(r1, 0.f) & 0xffffu; const unsigned c = pk2(r1 - bflo(bb), 0.f) & 0xffffu;
            if (h) { q1[2] = a | (bb << 16); q1[3] = c; }
        }
        o = zero;
        f32x16 sA, sB; u32x4 k0, k1, v0, v1, vn0, vn1;
        sA = mfma(lds128(rd), q0, zero); sA = mfma(lds128(rd + 1024), q1, sA);
        k0 = lds128(rd + 4096); k1 = lds128(rd + 4096 + 1024);
        v0 = lds128(rd + 2048); v1 = lds128(rd + 3072);
#define STEP(SC, SN, KOFF, VOFF, DOQK, DOK, DOV) do { \
        if (DOV) { vn0 = lds128(rd + (VOFF) + 2048); vn1 = lds128(rd + (VOFF) + 3072); } SBAR(); \
        if (DOQK) SN = mfma(k0, q0, zero); EXP4(SC, 0); PIN(SC); SBAR(); \
        if (DOQK) SN = mfma(k1, q1, SN); if (DOK) { k0 = lds128(rd + (KOFF)); k1 = lds128(rd + (KOFF) + 1024); } EXP4(SC, 4); SC[8] = __builtin_amdgcn_exp2f(SC[8]); SC[9] = __builtin_amdgcn_exp2f(SC[9]); PIN(SC); SBAR(); \
        o = pv(v0, SC, 0, o); SC[10] = __builtin_amdgcn_exp2f(SC[10]); SC[11] = __builtin_amdgcn_exp2f(SC[11]); EXP4(SC, 12); PIN(SC); SBAR(); \
        o = pv(v1, SC, 1, o); v0 = vn0; v1 = vn1; SBAR(); } while (0)
        for (int st = 0; st < 7; ++st) {
            const int cur = (st & 3) * 32768, nxt = ((st + 1) & 3) * 32768;
            STEP(sA, sB, cur + 2 * 4096, cur + 1 * 4096, true, true, true);
            STEP(sB, sA, cur + 3 * 4096, cur + 2 * 4096, true, true, true);
            if (st < 6) { WAITV(4); } else { WAITV(0); }
            BARRIER();
            if (st < 5) issue(st + 3);
            STEP(sA, sB, nxt + 0 * 4096, cur + 3 * 4096, true, true, true);
            STEP(sB, sA, nxt + 1 * 4096, nxt + 0 * 4096, true, true, true);
        }
        {
            const int cur = 3 * 32768;
            STEP(sA, sB, cur + 2 * 4096, cur + 1 * 4096, true, true, true);
            STEP(sB, sA, cur + 3 * 4096, cur + 2 * 4096, true, true, true);
            STEP(sA, sB, 0, cur + 3 * 4096, true, false, true);
            STEP(sB, sA, 0, 0, false, false, false);
        }
        STAMP(1, 4);
        LAS f32x4* red = (LAS f32x4*)(lds + K2_RED + qt * 4096) + lane;
        LAS int* flag = (LAS int*)(lds + K2_RED + 4 * 4096);
        if (threadIdx.x == 0) *flag = 0;
        if (kh) {
#pragma unroll
            for (int g = 0; g < 4; ++g) red[g * 64] = (f32x4){o[4 * g], o[4 * g + 1], o[4 * g + 2], o[4 * g + 3]};
        }
        BARRIER();
        if (!kh) {
#pragma unroll
            for (int g = 0; g < 4; ++g) { f32x4 t = red[g * 64]; o[4 * g] += t[0]; o[4 * g + 1] += t[1]; o[4 * g + 2] += t[2]; o[4 * g + 3] += t[3]; }
            float amax = 0.f;
#pragma unroll
            for (int r = 0; r < 16; ++r) amax = fmaxf(amax, fabsf(o[r]));
            const bool bad = !(amax <= 3e38f) || (h && !(o[12] <= 1e18f));
            if (__any(bad)) *flag = 1;
        }
        BARRIER();
        if (*flag == 0) break;
        BARRIER();
    }
    if (!kh) {
        const float lo_ = swap32(o[12]);
        const float rl = __builtin_amdgcn_rcpf(h ? o[12] : lo_);
        o = o * rl;
        char* dst = ws + WS_O + (size_t)((rb * 4 + qt) * 32 + head * 2) * 1024 + lane * 16;
        *(u32x4*)dst = pack8(o, 0); *(u32x4*)(dst + 1024) = pack8(o, 1);
    }
    STAMP(1, 1);
}

constexpr int K3_LDS = 65536 + 16384;
__global__ __launch_bounds__(512, 2) void k3_wo(const u16* __restrict__ x, char* __restrict__ ws) {
    extern __shared__ __attribute__((aligned(16))) char smem[];
    LAS char* lds = (LAS char*)smem;
    const int lane = threadIdx.x & 63, w = __builtin_amdgcn_readfirstlane(threadIdx.x >> 6);
    const int b = blockIdx.x, jj = b >> 3, rb = 4 * (b & 7) + (jj & 3), ct = jj >> 2;
    const int nt2 = w & 1, rt = (w >> 1) & 1, kh = w >> 2, i = lane & 31, h = lane >> 5;
    STAMP(2, 0);
    const char* src = ws + WS_O + (size_t)rb * 65536 + w * 8192 + lane * 16;
#pragma unroll
    for (int p = 0; p < 8; ++p) glds16(src + p * 1024, (unsigned)(uintptr_t)lds + w * 8192 + p * 1024);
    const char* asrc = ws + WS_WO + (size_t)((2 * ct + nt2) * 32 + 16 * kh) * 1024 + lane * 16;
    u32x4 a[16];
#pragma unroll
    for (int k = 0; k < 16; ++k) a[k] = *(const u32x4*)(asrc + k * 1024);
    u32x2 xr[4];
    const u16* xp = x + (size_t)(rb * 64 + rt * 32 + i) * D_ + ct * 64 + nt2 * 32 + 4 * h;
#pragma unroll
    for (int g = 0; g < 4; ++g) xr[g] = *(const u32x2*)(xp + 8 * g);
    STAMP(2, 2);
    WAITV(0);
    STAMP(2, 3);
    BARRIER();
    STAMP(2, 4);
    f32x16 acc = {};
#pragma unroll
    for (int k = 0; k < 16; ++k) acc = mfma(a[k], lds128(lds + (rt * 32 + kh * 16 + k) * 1024 + lane * 16), acc);
    LAS f32x4* red = (LAS f32x4*)(lds + 65536 + (w & 3) * 4096) + lane;
    if (kh) {
#pragma unroll
        for (int g = 0; g < 4; ++g) red[g * 64] = (f32x4){acc[4 * g], acc[4 * g + 1], acc[4 * g + 2], acc[4 * g + 3]};
    }
    STAMP(2, 5);
    BARRIER();
    if (!kh) {
        float s = 0.f;
#pragma unroll
        for (int g = 0; g < 4; ++g) {
            f32x4 t = red[g * 64];
            acc[4 * g] += t[0] + bflo(xr[g][0]); acc[4 * g + 1] += t[1] + bfhi(xr[g][0]); acc[4 * g + 2] += t[2] + bflo(xr[g][1]); acc[4 * g + 3] += t[3] + bfhi(xr[g][1]);
            s += (acc[4 * g] + acc[4 * g + 1]) + (acc[4 * g + 2] + acc[4 * g + 3]);
        }
        const float mu = sum32(s) * (1.f / 32.f);
        float m2 = 0.f;
#pragma unroll
        for (int r = 0; r < 16; ++r) { const float d = acc[r] - mu; m2 += d * d; }
        m2 = sum32(m2);
        const int rtile = rb * 2 + rt, nt = 2 * ct + nt2;
        if (!h) *(f32x2*)(ws + WS_ST + (size_t)((rtile * 14 + nt) * 32 + i) * 8) = (f32x2){mu, m2};
        char* dst = ws + WS_H + (size_t)(rtile * 28 + 2 * nt) * 1024 + lane * 16;
        *(u32x4*)dst = pack8(acc, 0); *(u32x4*)(dst + 1024) = pack8(acc, 1);
    }
    STAMP(2, 1);
}

constexpr int K4_LDS = 57344;
DI float gelu_tanh(float x) {
    const float t = __builtin_fmaf(x * x, -0.1029432397f, -2.3022081981f);
    const float e = __builtin_amdgcn_exp2f(x * t);
    return x * __builtin_amdgcn_rcpf(1.f + e);
}
__global__ __launch_bounds__(512, 2) void k4_mlp1(const u16* __restrict__ b1, char* __restrict__ ws) {
    extern __shared__ __attribute__((aligned(16))) char smem[];
    LAS char* lds = (LAS char*)smem;
    const int lane = threadIdx.x & 63, w = __builtin_amdgcn_readfirstlane(threadIdx.x >> 6);
    const int b = blockIdx.x, jj = b >> 3, rb = 4 * (b & 7) + (jj & 3), fb = jj >> 2;
    const int i = lane & 31, h = lane >> 5, ft = fb * 8 + w;
    STAMP(3, 0);
    const char* src = ws + WS_H + (size_t)rb * 57344 + w * 7168 + lane * 16;
#pragma unroll
    for (int p = 0; p < 7; ++p) glds16(src + p * 1024, (unsigned)(uintptr_t)lds + w * 7168 + p * 1024);
    f32x2 st[14];
    const char* sp = ws + WS_ST + (size_t)((rb * 2 + h) * 14 * 32 + i) * 8;
#pragma unroll
    for (int n = 0; n < 14; ++n) st[n] = *(const f32x2*)(sp + n * 256);
    const unsigned bias = b1[ft * 32 + i];
    const char* asrc = ws + WS_W1 + (size_t)ft * 28 * 1024 + lane * 16;
    u32x4 a[28];
#pragma unroll
    for (int k = 0; k < 28; ++k) a[k] = *(const u32x4*)(asrc + k * 1024);
    float ms = 0.f;
#pragma unroll
    for (int n = 0; n < 14; ++n) ms += st[n][0];
    const float mean = ms * (1.f / 14.f);
    float m2 = 0.f;
#pragma unroll
    for (int n = 0; n < 14; ++n) { const float d = st[n][0] - mean; m2 += st[n][1] + 32.f * d * d; }
    const float var = m2 * (1.f / D_);
    const float rs1 = __builtin_amdgcn_rsqf(var + EPS);
    const float rs2 = __builtin_amdgcn_rsqf(var * rs1 * rs1 + EPS);
    const float rsv = rs1 * rs2;
    const float mo = swap32(mean), ro = swap32(rsv);
    float meanr[2] = {h ? mo : mean, h ? mean : mo}, rsr[2] = {h ? ro : rsv, h ? rsv : ro};
    STAMP(3, 2);
    asm volatile("s_waitcnt vmcnt(28)" ::: "memory"); BARRIER();
    STAMP(3, 3);
    f32x16 acc[2] = {{}, {}};
    float cs = 0.f;
#pragma unroll
    for (int k = 0; k < 28; ++k) {
        acc[0] = mfma(a[k], lds128(lds + k * 1024 + lane * 16), acc[0]);
        acc[1] = mfma(a[k], lds128(lds + (28 + k) * 1024 + lane * 16), acc[1]);
#pragma unroll
        for (int e = 0; e < 4; ++e) cs = dot2(a[k][e], 0x3f803f80u, cs);
    }
    STAMP(3, 4);
    cs = sum32(cs);
    unsigned ch, cl; split2(cs, ch, cl);
    const u32x4 ae = {h ? 0u : (ch | (cl << 16)), h ? 0u : (ch | (cl << 16)), h ? 0u : (bias | (bias << 16)), 0u};
#pragma unroll
    for (int rt = 0; rt < 2; ++rt) {
        unsigned mh, ml, sh, sl; split2(-meanr[rt], mh, ml); split2(__builtin_amdgcn_rcpf(rsr[rt]), sh, sl);
        const u32x4 be = {h ? 0u : (mh | (mh << 16)), h ? 0u : (ml | (ml << 16)), h ? 0u : (sh | (sl << 16)), 0u};
        acc[rt] = mfma(ae, be, acc[rt]);
#pragma unroll
        for (int r = 0; r < 16; ++r) acc[rt][r] = gelu_tanh(acc[rt][r] * rsr[rt]);
        char* dst = ws + WS_HID + (size_t)((rb * 2 + rt) * 112 + 2 * ft) * 1024 + lane * 16;
        *(u32x4*)dst = pack8(acc[rt], 0); *(u32x4*)(dst + 1024) = pack8(acc[rt], 1);
    }
    STAMP(3, 1);
}

constexpr int K5_LDS = 131072;
__global__ __launch_bounds__(512, 2) void k5_mlp2(const u16* __restrict__ b2, char* __restrict__ ws, u16* __restrict__ out) {
    extern __shared__ __attribute__((aligned(16))) char smem[];
    LAS char* lds = (LAS char*)smem;
    const int lane = threadIdx.x & 63, w = __builtin_amdgcn_readfirstlane(threadIdx.x >> 6);
    const int b = blockIdx.x, jj = b >> 3, rb = 4 * (b & 7) + (jj & 3), ct = jj >> 2;
    const int nt2 = w & 1, kq = w >> 1, i = lane & 31, h = lane >> 5;
    STAMP(4, 0);
    const char* src = ws + WS_HID + (size_t)((rb * 2 + nt2) * 112 + kq * 28) * 1024 + lane * 16;
    const unsigned ldst = (unsigned)(uintptr_t)lds + w * 4096;
    auto issue = [&](int st) {
#pragma unroll
        for (int p = 0; p < 4; ++p) glds16(src + (st * 4 + p) * 1024, ldst + (st & 3) * 32768 + p * 1024);
    };
    const char* asrc = ws + WS_W2 + (size_t)((2 * ct + nt2) * 112 + kq * 28) * 1024 + lane * 16;
    u32x4 a[28];
#pragma unroll
    for (int k = 0; k < 28; ++k) a[k] = *(const u32x4*)(asrc + k * 1024);
    const int prt = kq >> 1, ps = kq & 1, nt = 2 * ct + nt2;
    const u32x4 hres = *(const u32x4*)(ws + WS_H + (size_t)((rb * 2 + prt) * 28 + 2 * nt + ps) * 1024 + lane * 16);
    const u32x2 bb0 = *(const u32x2*)(b2 + nt * 32 + ps * 16 + 4 * h), bb1 = *(const u32x2*)(b2 + nt * 32 + ps * 16 + 8 + 4 * h);
    SBAR();
    issue(0); issue(1); issue(2); issue(3);
    f32x16 acc[2] = {{}, {}};
#pragma unroll
    for (int st = 0; st < 7; ++st) {
        if (st == 0) WAITV(12); else if (st < 5) WAITV(8); else if (st == 5) WAITV(4); else WAITV(0);
        BARRIER();
        if (st >= 1 && st + 3 < 7) issue(st + 3);
        LAS char* base = lds + (st & 3) * 32768 + kq * 8192 + lane * 16;
#pragma unroll
        for (int k = 0; k < 4; ++k) {
            acc[0] = mfma(a[st * 4 + k], lds128(base + k * 1024), acc[0]);
            acc[1] = mfma(a[st * 4 + k], lds128(base + 4096 + k * 1024), acc[1]);
        }
    }
    BARRIER();
    LAS f32x4* red = (LAS f32x4*)lds;
#pragma unroll
    for (int p = 0; p < 4; ++p) {
        if (p != kq) {
            const f32x16& t = acc[p >> 1]; const int s = p & 1;
            red[(((nt2 * 4 + p) * 4 + kq) * 2 + 0) * 64 + lane] = (f32x4){t[8 * s], t[8 * s + 1], t[8 * s + 2], t[8 * s + 3]};
            red[(((nt2 * 4 + p) * 4 + kq) * 2 + 1) * 64 + lane] = (f32x4){t[8 * s + 4], t[8 * s + 5], t[8 * s + 6], t[8 * s + 7]};
        }
    }
    f32x4 r0, r1;
    {
        const f32x16 t = prt ? acc[1] : acc[0];
        r0 = ps ? (f32x4){t[8], t[9], t[10], t[11]} : (f32x4){t[0], t[1], t[2], t[3]};
        r1 = ps ? (f32x4){t[12], t[13], t[14], t[15]} : (f32x4){t[4], t[5], t[6], t[7]};
    }
    BARRIER();
#pragma unroll
    for (int q = 0; q < 4; ++q) {
        if (q != kq) { r0 += red[(((nt2 * 4 + kq) * 4 + q) * 2 + 0) * 64 + lane]; r1 += red[(((nt2 * 4 + kq) * 4 + q) * 2 + 1) * 64 + lane]; }
    }
    unsigned p0 = pk2(r0[0] + bflo(bb0[0]) + bflo(hres[0]), r0[1] + bfhi(bb0[0]) + bfhi(hres[0]));
    unsigned p1 = pk2(r0[2] + bflo(bb0[1]) + bflo(hres[1]), r0[3] + bfhi(bb0[1]) + bfhi(hres[1]));
    unsigned p2 = pk2(r1[0] + bflo(bb1[0]) + bflo(hres[2]), r1[1] + bfhi(bb1[0]) + bfhi(hres[2]));
    unsigned p3 = pk2(r1[2] + bflo(bb1[1]) + bflo(hres[3]), r1[3] + bfhi(bb1[1]) + bfhi(hres[3]));
    auto s0 = __builtin_amdgcn_permlane32_swap(p0, p2, false, false);
    auto s1 = __builtin_amdgcn_permlane32_swap(p1, p3, false, false);
    u16* dst = out + (size_t)(rb * 64 + prt * 32 + i) * D_ + nt * 32 + ps * 16 + 8 * h;
    *(u32x4*)dst = (u32x4){s0[0], s1[0], s0[1], s1[1]};
    STAMP(4, 1);
}

extern "C" void kernel_launch(void* const* d_in, const int* in_sizes, int n_in, void* d_out, int out_size, void* d_ws, size_t ws_size, hipStream_t stream) {
    const u16* x = (const u16*)d_in[0]; const u16* wqkv = (const u16*)d_in[1]; const u16* wo = (const u16*)d_in[2];
    const u16* w1 = (const u16*)d_in[4]; const u16* b1 = (const u16*)d_in[5]; const u16* w2 = (const u16*)d_in[6]; const u16* b2 = (const u16*)d_in[7];
    char* ws = (char*)d_ws;
    (void)hipFuncSetAttribute((const void*)k1_qkv, hipFuncAttributeMaxDynamicSharedMemorySize, K1_LDS);
    (void)hipFuncSetAttribute((const void*)k2_attn, hipFuncAttributeMaxDynamicSharedMemorySize, K2_LDS);
    (void)hipFuncSetAttribute((const void*)k3_wo, hipFuncAttributeMaxDynamicSharedMemorySize, K3_LDS);
    (void)hipFuncSetAttribute((const void*)k5_mlp2, hipFuncAttributeMaxDynamicSharedMemorySize, K5_LDS);
    k1_qkv<<<256, 512, K1_LDS, stream>>>(x, wqkv, wo, w1, w2, ws);
    k2_attn<<<256, 512, K2_LDS, stream>>>(ws);
    k3_wo<<<224, 512, K3_LDS, stream>>>(x, ws);
    k4_mlp1<<<224, 512, K4_LDS, stream>>>(b1, ws);
    k5_mlp2<<<224, 512, K5_LDS, stream>>>(b2, ws, (u16*)d_out);
}
```

```cpp
#include <hip/hip_runtime.h>
#include <stdint.h>

typedef unsigned short u16;
typedef unsigned long long u64;
typedef __bf16 bf16x2 __attribute__((ext_vector_type(2)));
typedef __bf16 bf16x8 __attribute__((ext_vector_type(8)));
typedef __bf16 bf16x16 __attribute__((ext_vector_type(16)));
typedef float f32x2 __attribute__((ext_vector_type(2)));
typedef float f32x4 __attribute__((ext_vector_type(4)));
typedef float f32x8 __attribute__((ext_vector_type(8)));
typedef float f32x16 __attribute__((ext_vector_type(16)));
typedef unsigned u32x2 __attribute__((ext_vector_type(2)));
typedef unsigned u32x4 __attribute__((ext_vector_type(4)));
typedef short s16x4 __attribute__((ext_vector_type(4)));
typedef __amdgpu_buffer_rsrc_t rsrc_t;
#define LAS __attribute__((address_space(3)))
#define DI __device__ __forceinline__
#define SBAR() __builtin_amdgcn_sched_barrier(0)
#define PIN(x) asm volatile("" : "+v"(x))

constexpr int CODE_BYTES = 28672, CODE_BACK = 0;
#define FBPOS 13
#define W1MODE 1
constexpr int D_ = 448, F_ = 1792;
constexpr float EPS = 1e-5f;
constexpr int WS_EPOCH = 0;
constexpr int WS_F1 = 1024;
constexpr int WS_FR = WS_F1 + 96 * 128;
constexpr int WS_F2 = WS_FR + 2048;
constexpr int WS_F3 = WS_F2 + 32 * 128;
constexpr int WS_F4 = WS_F3 + 32 * 128;
constexpr int WS_ST = 32768;
constexpr int WS_Q = WS_ST + 64 * 14 * 256;
constexpr int WS_KV = WS_Q + 16 * 64 * 2048;
constexpr int WS_O = WS_KV + 16 * 64 * 4096;
constexpr int WS_H = WS_O + 64 * 32 * 1024;
constexpr int WS_W1 = WS_H + 64 * 28 * 1024;
constexpr int WS_W2 = WS_W1 + 56 * 28 * 1024;
constexpr int WS_WO = WS_W2 + 14 * 112 * 1024;
constexpr int WS_HID = WS_WO + 14 * 32 * 1024;
constexpr int WS_END = WS_HID + 64 * 112 * 1024;
static_assert(WS_F4 + 64 * 128 <= WS_ST, "flags overlap");

constexpr int XCH = 16384, WCH = 12288, CHB = XCH + WCH;
constexpr int L_MISC = 5 * CHB;
constexpr int L_CS = L_MISC, L_XE = L_MISC + 3072, L_CW = L_XE + 4096, L_RT = L_CW + 768;
constexpr int L_VT = 2 * CHB;
constexpr int L_RED = 131072;
constexpr int L_FLAG = L_RED + 16384;
constexpr int L_HS = L_RED + 16384 + 64 + 1024;
constexpr int L_CNT = 163840 - 1024 - 64;
constexpr int L_DUMP = 163840 - 1024;
constexpr int LDS_BYTES = 163840;

#define STAMP_DECL
#define STAMP(i)
#define CSTAMP(i)
#define STAMP_OUT()

DI f32x16 mfma(u32x4 a, u32x4 b, f32x16 c) {
    return __builtin_amdgcn_mfma_f32_32x32x16_bf16(__builtin_bit_cast(bf16x8, a), __builtin_bit_cast(bf16x8, b), c, 0, 0, 0);
}
DI f32x4 mfma16(u32x4 a, u32x4 b, f32x4 c) {
    return __builtin_amdgcn_mfma_f32_16x16x32_bf16(__builtin_bit_cast(bf16x8, a), __builtin_bit_cast(bf16x8, b), c, 0, 0, 0);
}
DI f32x4 mfma4(unsigned a0, unsigned a1, unsigned b0, unsigned b1, f32x4 c) {
    return __builtin_amdgcn_mfma_f32_4x4x4bf16_1k(__builtin_bit_cast(s16x4, (u32x2){a0, a1}), __builtin_bit_cast(s16x4, (u32x2){b0, b1}), c, 0, 0, 0);
}
DI f32x16 pv(u32x4 v, const f32x16& p, int s, f32x16 o) {
    f32x8 ph = {p[8 * s], p[8 * s + 1], p[8 * s + 2], p[8 * s + 3], p[8 * s + 4], p[8 * s + 5], p[8 * s + 6], p[8 * s + 7]};
    return __builtin_amdgcn_smfmac_f32_32x32x32_bf16(__builtin_bit_cast(bf16x8, v), __builtin_bit_cast(bf16x16, ph), o, 0xDDDD, 0, 0);
}
DI unsigned pk2(float lo, float hi) { f32x2 v = {lo, hi}; return __builtin_bit_cast(unsigned, __builtin_convertvector(v, bf16x2)); }
DI float bflo(unsigned u) { return __uint_as_float(u << 16); }
DI float bfhi(unsigned u) { return __uint_as_float(u & 0xffff0000u); }
DI float dot2(unsigned a, unsigned b, float c) { return __builtin_amdgcn_fdot2_f32_bf16(__builtin_bit_cast(bf16x2, a), __builtin_bit_cast(bf16x2, b), c, false); }
DI void both32(float v, float& lo, float& hi) {
    auto r = __builtin_amdgcn_permlane32_swap(__float_as_uint(v), __float_as_uint(v), false, false);
    lo = __uint_as_float(r[0]); hi = __uint_as_float(r[1]);
}
DI float sum32(float v) { float a, b; both32(v, a, b); return a + b; }
DI float max32(float v) { float a, b; both32(v, a, b); return fmaxf(a, b); }
DI u32x4 lds128(const LAS char* p) { return *(const LAS u32x4*)p; }
DI u32x2 ldstr(const LAS char* p) { return __builtin_bit_cast(u32x2, __builtin_amdgcn_ds_read_tr16_b64_v4i16((LAS s16x4*)p)); }
DI void split2(float v, unsigned& hi, unsigned& lo) { hi = pk2(v, 0.f) & 0xffffu; lo = pk2(v - bflo(hi), 0.f) & 0xffffu; }
DI u32x4 pack8(const f32x16& a, int s) {
    u32x4 r;
    r[0] = pk2(a[8 * s + 0], a[8 * s + 1]); r[1] = pk2(a[8 * s + 2], a[8 * s + 3]);
    r[2] = pk2(a[8 * s + 4], a[8 * s + 5]); r[3] = pk2(a[8 * s + 6], a[8 * s + 7]);
    return r;
}
DI void dma(rsrc_t rs, int voff, int soff, unsigned lds_addr) {
    asm volatile("s_mov_b32 m0, %0\n\ts_nop 3\n\tbuffer_load_dwordx4 %1, %2, %3 offen lds" :: "s"(lds_addr), "v"(voff), "s"(rs), "s"(soff) : "memory");
}
DI void dma_c(rsrc_t rs, int voff, int soff, unsigned lds_addr) {
    asm volatile("s_mov_b32 m0, %0\n\ts_nop 3\n\tbuffer_load_dwordx4 %1, %2, %3 offen sc1 lds" :: "s"(lds_addr), "v"(voff), "s"(rs), "s"(soff) : "memory");
}
DI void dma_c4(rsrc_t rs, int voff, int soff, unsigned lds_addr) {
    asm volatile("s_mov_b32 m0, %0\n\ts_nop 3\n\tbuffer_load_dwordx4 %1, %2, %3 offen sc1 lds\n\tbuffer_load_dwordx4 %1, %2, %3 offen offset:1024 sc1 lds\n\t"
                 "buffer_load_dwordx4 %1, %2, %3 offen offset:2048 sc1 lds\n\tbuffer_load_dwordx4 %1, %2, %3 offen offset:3072 sc1 lds"
                 :: "s"(lds_addr), "v"(voff), "s"(rs), "s"(soff) : "memory");
}
DI u32x4 ld_c(rsrc_t rs, int voff, int soff) { return __builtin_amdgcn_raw_buffer_load_b128(rs, voff, soff, 16); }
DI void st_c(u32x4 v, rsrc_t rs, int voff, int soff) { __builtin_amdgcn_raw_buffer_store_b128(v, rs, voff, soff, 16); }
DI u32x2 ld_flag(rsrc_t rs, int voff) { asm volatile("" ::: "memory"); return __builtin_amdgcn_raw_buffer_load_b64(rs, voff, 0, 16); }
DI void st_flag(rsrc_t rs, int voff, u32x2 e) { __builtin_amdgcn_raw_buffer_store_b64(e, rs, voff, 0, 16); }
template <int N> DI void waitv() {
    asm volatile("s_waitcnt vmcnt(%0)" :: "n"(N) : "memory");
    if (N == 0) __builtin_amdgcn_s_waitcnt(0x0F70);
}
#define BAR() asm volatile("s_barrier" ::: "memory")
#define BARRIER() asm volatile("s_waitcnt lgkmcnt(0)\n\ts_barrier" ::: "memory")
DI bool flag_ok(u32x2 f, u32x2 e) { return __all(f[0] == e[0] && f[1] == e[1]); }
DI void wait_flags(rsrc_t rs, int voff, u32x2 e) {
#pragma unroll 1
    for (int spin = 0; spin < (1 << 18); ++spin) {
        if (flag_ok(ld_flag(rs, voff), e)) break;
        __builtin_amdgcn_s_sleep(1);
    }
    asm volatile("" ::: "memory");
}
DI void arrive(LAS char* lds, int cnt, int n, rsrc_t rs, int foff, u32x2 e, int lane) {
    if (lane == 0) {
        const unsigned old = __hip_atomic_fetch_add((LAS unsigned*)(lds + L_CNT) + cnt, 1u, __ATOMIC_RELAXED, __HIP_MEMORY_SCOPE_WORKGROUP);
        if (old == (unsigned)(n - 1)) st_flag(rs, foff, e);
    }
}
template <int OFF> DI void dma_co(rsrc_t rs, int voff, int soff, unsigned lds_addr) {
    asm volatile("s_mov_b32 m0, %0\n\ts_nop 3\n\tbuffer_load_dwordx4 %1, %2, %3 offen offset:%4 sc1 lds" :: "s"(lds_addr), "v"(voff), "s"(rs), "s"(soff), "n"(OFF) : "memory");
}
DI u32x4 ld_c_async(rsrc_t rs, int voff, int soff) { u32x4 r; asm volatile("s_nop 4\n\tbuffer_load_dwordx4 %0, %1, %2, %3 offen sc1" : "=v"(r) : "v"(voff), "s"(rs), "s"(soff) : "memory"); return r; }
DI u32x2 ld_flag_async(rsrc_t rs, int voff) { u32x2 r; asm volatile("buffer_load_dwordx2 %0, %1, %2, 0 offen sc1" : "=v"(r) : "v"(voff), "s"(rs) : "memory"); return r; }
DI float gelu_tanh(float x) {
    const float t = __builtin_fmaf(x * x, -0.1029432397f, -2.3022081981f);
    const float e = __builtin_amdgcn_exp2f(x * t);
    return x * __builtin_amdgcn_rcpf(1.f + e);
}

__global__ __launch_bounds__(512, 2) void fused_block(const u16* __restrict__ x, const u16* __restrict__ wqkv, const u16* __restrict__ wo,
                                                       const u16* __restrict__ w1, const u16* __restrict__ b1, const u16* __restrict__ w2,
                                                       const u16* __restrict__ b2, u16* __restrict__ out, char* __restrict__ ws) {
    extern __shared__ __attribute__((aligned(16))) char smem[];
    LAS char* lds = (LAS char*)smem;
    const unsigned lbase = (unsigned)(uintptr_t)lds;
    const int lane = threadIdx.x & 63, w = __builtin_amdgcn_readfirstlane(threadIdx.x >> 6);
    const int i = lane & 31, h = lane >> 5, l16 = lane * 16;
    const int b = blockIdx.x, xcd = b & 7, jj = b >> 3;
    STAMP_DECL; STAMP(0);
    const int rb = 4 * (xcd >> 1) + (jj & 3), ct = 8 * (xcd & 1) + (jj >> 2);
    const int tt = w & 3;
        const rsrc_t rx = __builtin_amdgcn_make_buffer_rsrc((void*)x, 0, 2048 * 896, 0x00020000);
        const rsrc_t rq = __builtin_amdgcn_make_buffer_rsrc((void*)wqkv, 0, 448 * 2688, 0x00020000);
        const int vx0 = (lane >> 3) * 896 + (((lane & 7) ^ (lane >> 4)) << 4), vx1 = vx0 ^ 64;
        int vw[3]; u64 mk[3];
#pragma unroll
        for (int p = 0; p < 3; ++p) { const int q = (3 * tt + p) * 64 + lane, row = (q * 43691) >> 19; vw[p] = row * (2688 - 192) + q * 16; mk[p] = __ballot(q - row * 12 != 11); }
        int cg = (ct + 2 * rb) % 7;
        const int sx0 = (rb * 128 + 32 * tt) * 896, sw0 = (168 * ct) & ~15;
        unsigned ldn = lbase;
        auto issue1 = [&](int p) {
            if (p < 4) dma(rx, (p & 1) ? vx1 : vx0, sx0 + p * 7168 + cg * 128, ldn + (4 * tt + p) * 1024);
            else
                asm volatile("s_mov_b32 m0, %0\n\ts_mov_b64 exec, %4\n\ts_nop 2\n\tbuffer_load_dwordx4 %1, %2, %3 offen lds\n\ts_mov_b64 exec, -1"
                             :: "s"(ldn + XCH + (3 * tt + p - 4) * 1024), "v"(vw[p - 4]), "s"(rq), "s"(sw0 + cg * (64 * 2688)), "s"(mk[p - 4]) : "memory");
        };
    if (w >= 4) {
#pragma unroll 1
        for (int c = 0; c < 2; ++c) {
#pragma unroll
            for (int p = 0; p < 7; ++p) issue1(p);
            cg = cg == 6 ? 0 : cg + 1; ldn += CHB;
        }
    }
    STAMP(28);
    SBAR();
    const rsrc_t rw = __builtin_amdgcn_make_buffer_rsrc(ws, 0, WS_END, 0x00020000);
    const f32x16 zero = {};
    const unsigned ONES = 0x3f803f80u;
    u32x2 ep;
    u32x4 rte[2], rto[2]; int rdst[2], rts[2];
    if (w >= 4) {
        __builtin_amdgcn_s_setprio(3);
        const int r16 = lane & 15, g16 = lane >> 4;
        const LAS char* xp = lds + (32 * tt + r16) * 128;
        const LAS char* wp = lds + XCH + (32 * (tt >> 1) + 8 * g16 + (r16 >> 2)) * 192 + ((168 * ct) & 15) + ((tt & 1) * 16 + (lane & 3) * 4) * 2;
        const u32x4 ones4 = {ONES, ONES, ONES, ONES};
        f32x4 sq[2] = {{}, {}}, cs[3] = {{}, {}, {}};
        u32x4 xf[4], wf[3];
        int sl = 0;
        auto rd = [&](int p) {
            if (p < 4) xf[p] = lds128(xp + sl + (p & 1) * 2048 + ((((p >> 1) * 4 + g16) ^ (r16 >> 1)) << 4));
            else { const u32x2 t0 = ldstr(wp + sl + (p - 4) * 56), t1 = ldstr(wp + sl + (p - 4) * 56 + 768); wf[p - 4] = (u32x4){t0[0], t0[1], t1[0], t1[1]}; }
            SBAR();
        };
        auto acc_ = [&](int p) {
            if (p < 4) sq[p & 1] = mfma16(xf[p], xf[p], sq[p & 1]);
            else cs[p - 4] = mfma16(ones4, wf[p - 4], cs[p - 4]);
            SBAR();
        };
        waitv<7>(); BAR();
        STAMP(1);
#pragma unroll
        for (int p = 0; p < 7; ++p) { issue1(p); rd(p); }
        cg = cg == 6 ? 0 : cg + 1; ldn += CHB;
#pragma unroll
        for (int p = 0; p < 7; ++p) issue1(p);
        cg = cg == 6 ? 0 : cg + 1; ldn += CHB;
#pragma unroll 1
        for (int c = 4; c < 7; ++c) {
            waitv<14>(); BAR();
            sl += CHB;
#pragma unroll
            for (int p = 0; p < 7; ++p) { issue1(p); acc_(p); rd(p); }
            cg = cg == 6 ? 0 : cg + 1; ldn = ldn == lbase + 4 * CHB ? lbase : ldn + CHB;
        }
        unsigned rv[2][8];
        STAMP(25);
#pragma unroll
        for (int n = 0; n < 3; ++n) {
            rsrc_t rr; int rst, rs0, v0, v1;
            if (n < 2) {
                const u32x4 pa = lds128(lds + L_RT + (tt * 2 + n) * 32), pb = lds128(lds + L_RT + (tt * 2 + n) * 32 + 16);
                const u64 src = (u64)(unsigned)__builtin_amdgcn_readfirstlane(pa[0]) | ((u64)(unsigned)__builtin_amdgcn_readfirstlane(pa[1]) << 32);
                rr = __builtin_amdgcn_make_buffer_rsrc((void*)src, 0, __builtin_amdgcn_readfirstlane(pa[2]), 0x00020000);
                rst = __builtin_amdgcn_readfirstlane(pa[3]); rs0 = __builtin_amdgcn_readfirstlane(pb[0]);
                rdst[n] = __builtin_amdgcn_readfirstlane(pb[1]); rts[n] = __builtin_amdgcn_readfirstlane(pb[2]);
                v0 = 4 * h * rst + 4 * i; v1 = v0 | (h ? pb[3] : 0);
            }
            if (n == 0) waitv<14>(); else if (n == 1) waitv<15>(); else waitv<16>();
            if (n == 2) STAMP(26);
            BAR();
            if (n == 2) STAMP(27);
            sl = sl == 4 * CHB ? 0 : sl + CHB;
#pragma unroll
            for (int p = 0; p < 8; ++p) {
                if (p < 7) { acc_(p); rd(p); }
                if (n < 2) { rv[n][p] = __builtin_amdgcn_raw_buffer_load_b32(rr, p < 4 ? v0 : v1, rs0 + rst * (8 * (p >> 2) + (p & 3)), 0); SBAR(); }
            }
        }
#pragma unroll
        for (int p = 0; p < 7; ++p) acc_(p);
        LAS float* csb = (LAS float*)(lds + L_CS);
        if (lane < 16) {
#pragma unroll
            for (int s = 0; s < 3; ++s) csb[(s * 2 + (tt >> 1)) * 32 + (tt & 1) * 16 + lane] = cs[s][0];
        }
#pragma unroll
        for (int tb = 0; tb < 2; ++tb) {
            const float q01 = (lane & 1) ? sq[tb][1] : sq[tb][0], q23 = (lane & 1) ? sq[tb][3] : sq[tb][2];
            if (g16 == (r16 >> 2)) ((LAS float*)(lds + L_XE))[tt * 32 + tb * 16 + r16] = (lane & 2) ? q23 : q01;
        }
        STAMP(2);
        BARRIER();
        __builtin_amdgcn_s_setprio(0);
        ep = *(const LAS u32x2*)(lds + L_CNT + 32);
#pragma unroll
        for (int n = 0; n < 2; ++n) {
#pragma unroll
            for (int j = 0; j < 4; ++j) { rte[n][j] = __builtin_amdgcn_perm(rv[n][2 * j + 1], rv[n][2 * j], 0x05040100u); rto[n][j] = __builtin_amdgcn_perm(rv[n][2 * j + 1], rv[n][2 * j], 0x07060302u); }
            if (n == 0 || tt < 3) { st_c(rte[n], rw, l16, rdst[n]); st_c(rto[n], rw, l16, rdst[n] + rts[n]); }
        }
        STAMP(3);
        waitv<0>();
        STAMP(4);
        arrive(lds, 3, 4, rw, WS_FR + b * 8, ep, lane);
    } else {
        if (w == 0) {
            u64 pc; asm volatile("s_getpc_b64 %0" : "=s"(pc));
            const rsrc_t rc = __builtin_amdgcn_make_buffer_rsrc((void*)((pc & ~255ull) - CODE_BACK), 0, CODE_BYTES, 0x00020000);
            dma(rc, l16, jj * 1024, lbase + L_DUMP);
            if (lane < 8) ((LAS unsigned*)(lds + L_CNT))[lane] = 0u;
        }
        if (w == 3) {
            const u64 e = __builtin_bit_cast(u64, __builtin_amdgcn_raw_buffer_load_b64(rw, 0, WS_EPOCH, 16)) + 1;
            if (lane == 0) *(LAS u64*)(lds + L_CNT + 32) = e;
        }
        if (lane < 2) {
            const int u = 7 * b + (lane && tt < 3 ? tt + 4 : tt);
            const bool is1 = u < 784, is2 = u < 1568;
            const int q = is1 ? u : (is2 ? u - 784 : u - 1568), nks = is1 ? 28 : (is2 ? 112 : 32);
            const int win = is1 ? q / 28 : (is2 ? q / 112 : q >> 5), ks = q - win * nks;
            const int r0 = is2 ? 64 * (ks >> 2) + ((ks >> 1) & 1) + 32 * (ks & 1) : 28 * (ks >> 1) + 16 * (ks & 1);
            const u64 src = (u64)(is1 ? w1 : (is2 ? w2 : wo));
            LAS u32x4* pr = (LAS u32x4*)(lds + L_RT + (tt * 2 + lane) * 32);
            pr[0] = (u32x4){(unsigned)src, (unsigned)(src >> 32), (unsigned)(is2 ? 2 * D_ * F_ : 2 * D_ * D_), (unsigned)(is1 ? 4 * F_ : (is2 ? 4 * D_ : 2 * D_))};
            pr[1] = (u32x4){(unsigned)(r0 * (is1 ? 2 * F_ : 2 * D_) + win * 128), (unsigned)((is1 ? WS_W1 : (is2 ? WS_W2 : WS_WO)) + (q + win * nks) * 1024),
                            (unsigned)(nks * 1024), (!is2 && (ks & 1)) ? 0x40000000u : 0u};
        }
        const LAS char* xp[4];
#pragma unroll
        for (int ks = 0; ks < 4; ++ks) xp[ks] = lds + (32 * tt + i) * 128 + (((2 * ks + h) ^ ((i >> 1) & 7)) << 4);
        const int wcol = ((168 * ct) & 15) + (((lane >> 4) & 1) * 16 + (lane & 3) * 4) * 2;
        const LAS char* wp = lds + XCH + (8 * h + ((lane & 15) >> 2)) * 192 + wcol;
        const LAS char* wp2 = wp + ((lane & 19) == 19 ? 176 - wcol : 112);
        *(LAS u32x2*)(lds + tt * CHB + XCH + lane * 192 + 176) = (u32x2){ONES, ONES};
        if (w == 0) *(LAS u32x2*)(lds + 4 * CHB + XCH + lane * 192 + 176) = (u32x2){ONES, ONES};
        unsigned scl[3], pd2[3], pd3[3]; int dst[3];
#pragma unroll
        for (int s = 0; s < 3; ++s) {
            const int sec = 3 * ct + s, tl = (sec & 15) * 64 + rb * 4 + tt;
            scl[s] = sec < 16 ? __builtin_bit_cast(unsigned, 0.18898223650461361f * 1.4426950408889634f) : 0x3f800000u;
            pd2[s] = sec < 16 ? 0u : (sec < 32 ? ONES : 0x3f80u); pd3[s] = (sec >= 16 && sec < 32) ? 0x3f80u : 0u;
            dst[s] = sec < 16 ? WS_Q + tl * 2048 : WS_KV + tl * 4096 + (sec >= 32 ? 2048 : 0);
            asm volatile("" : "+s"(scl[s]), "+s"(pd2[s]), "+s"(pd3[s]), "+s"(dst[s]));
        }
        LAS char* vtw = lds + L_VT + w * 6912 + i * 72 + h * 8;
        const LAS char* vtr = lds + L_VT + w * 6912 + (8 * h + ((lane & 15) >> 2)) * 72 + ((lane >> 4) & 1) * 32 + (lane & 3) * 8;
        int f1o = WS_F1 + ((3 * ct + lane) * 2 + (rb >> 3)) * 128 + (rb & 7) * 8;
        PIN(vtw); PIN(vtr); PIN(f1o);
        f32x16 acc[3] = {zero, zero, zero};
        PIN(acc[0]); PIN(acc[1]); PIN(acc[2]);
        u32x4 xf[4], wf[4][3];
        auto rdw = [&](int ks, int s, const LAS char* p) {
            const u32x2 t0 = ldstr(p + ks * 3072 + (s & 1) * 56), t1 = ldstr(p + ks * 3072 + (s & 1) * 56 + 768);
            wf[ks][s] = (u32x4){t0[0], t0[1], t1[0], t1[1]};
        };
        BARRIER();
        STAMP(1);
#pragma unroll
        for (int ks = 0; ks < 4; ++ks) { xf[ks] = lds128(xp[ks]); rdw(ks, 0, wp); rdw(ks, 1, wp); rdw(ks, 2, wp2); SBAR(); }
        int sl = CHB;
#pragma unroll 1
        for (int c = 1; c < 7; ++c) {
            BAR();
            const LAS char* p = wp + sl; const LAS char* p2 = wp2 + sl;
#pragma unroll
            for (int ks = 0; ks < 4; ++ks)
#pragma unroll
                for (int s = 0; s < 3; ++s) {
                    acc[s] = mfma(wf[ks][s], xf[ks], acc[s]); SBAR();
                    if (s == 2) xf[ks] = lds128(xp[ks] + sl);
                    rdw(ks, s, s == 2 ? p2 : p); SBAR();
                }
            sl = sl == 4 * CHB ? 0 : sl + CHB;
        }
#pragma unroll
        for (int ks = 0; ks < 4; ++ks) { acc[2] = mfma(wf[ks][2], xf[ks], acc[2]); SBAR(); }
        acc[0] = mfma(wf[0][0], xf[0], acc[0]); SBAR();
        BAR();
        ep = *(const LAS u32x2*)(lds + L_CNT + 32);
        const float sqv = ((const LAS float*)(lds + L_XE))[tt * 32 + i];
        float cv[6];
#pragma unroll
        for (int s = 0; s < 6; ++s) cv[s] = ((const LAS float*)(lds + L_CS))[s * 32 + i];
        SBAR();
        acc[1] = mfma(wf[0][1], xf[0], acc[1]); SBAR();
        float lo_, sxv; both32(acc[2][12], lo_, sxv);
        const float mean = sxv * (1.f / D_);
        unsigned mh, ml; split2(-mean, mh, ml);
        u32x4 xe = {h ? 0u : (mh | (mh << 16)), h ? 0u : (ml | (ml << 16)), 0u, 0u};
        PIN(xe); SBAR();
        acc[0] = mfma(wf[1][0], xf[1], acc[0]); SBAR();
        float rstd = __builtin_amdgcn_rsqf(fmaxf(sqv * (1.f / D_) - mean * mean, 0.f) + EPS);
        PIN(rstd); SBAR();
        u32x4 we[3];
#pragma unroll
        for (int s = 0; s < 3; ++s) {
            if (s == 0) acc[1] = mfma(wf[1][1], xf[1], acc[1]); else acc[s - 1] = mfma(wf[2][s - 1], xf[2], acc[s - 1]);
            SBAR();
            unsigned ch, cl; split2(cv[2 * s] + cv[2 * s + 1], ch, cl);
            const unsigned cw = h ? 0u : (ch | (cl << 16));
            we[s] = (u32x4){cw, cw, 0u, 0u};
            PIN(we[s]); SBAR();
        }
        acc[0] = mfma(wf[3][0], xf[3], acc[0]); SBAR();
        acc[0] = mfma(we[0], xe, acc[0]); SBAR();
        acc[1] = mfma(wf[3][1], xf[3], acc[1]); SBAR();
        acc[1] = mfma(we[1], xe, acc[1]); SBAR();
        acc[2] = mfma(we[2], xe, acc[2]); SBAR();
        STAMP(2);
        u32x4 p0[3], p1[3];
#pragma unroll
        for (int s = 0; s < 3; ++s) {
            const f32x16 v = acc[s] * (rstd * __uint_as_float(scl[s]));
            p0[s] = pack8(v, 0); p1[s] = pack8(v, 1);
            if (h) { p1[s][2] = pd2[s]; p1[s][3] = pd3[s]; }
            if (3 * ct + s >= 32) {
                LAS u32x2* wq = (LAS u32x2*)(vtw + s * 2304);
                wq[0] = (u32x2){p0[s][0], p0[s][1]}; wq[2] = (u32x2){p0[s][2], p0[s][3]}; wq[4] = (u32x2){p1[s][0], p1[s][1]}; wq[6] = (u32x2){p1[s][2], p1[s][3]};
                const u32x2 t0 = ldstr(vtr + s * 2304), t1 = ldstr(vtr + s * 2304 + 288), t2 = ldstr(vtr + s * 2304 + 1152), t3 = ldstr(vtr + s * 2304 + 1440);
                p0[s] = (u32x4){t0[0], t0[1], t1[0], t1[1]}; p1[s] = (u32x4){t2[0], t2[1], t3[0], t3[1]};
            }
            if (s) { st_c(p0[s - 1], rw, l16, dst[s - 1]); st_c(p1[s - 1], rw, l16 + 1024, dst[s - 1]); }
        }
        st_c(p0[2], rw, l16, dst[2]); st_c(p1[2], rw, l16 + 1024, dst[2]);
        STAMP(3);
        waitv<0>();
        STAMP(4);
        unsigned old = 0;
        if (lane == 0) old = __hip_atomic_fetch_add((LAS unsigned*)(lds + L_CNT) + 4, 1u, __ATOMIC_RELAXED, __HIP_MEMORY_SCOPE_WORKGROUP);
        if (__builtin_amdgcn_readfirstlane(old) == 3 && lane < 3) st_flag(rw, f1o, ep);
    }
    ep[0] = __builtin_amdgcn_readfirstlane(ep[0]); ep[1] = __builtin_amdgcn_readfirstlane(ep[1]);

    const int head = 2 * xcd + (jj & 1), qb = jj >> 1, kh = w >> 2;
    const int r3 = 4 * xcd + (jj & 3), c3 = jj >= 28 ? 6 : (jj >> 2);
    u32x4 a1[28];
    const int w1s = WS_W1 + (8 * c3 + w) * 28 * 1024;
    f32x16 o;
    {
        const int kvs = WS_KV + head * 64 * 4096 + (kh * 32 + tt) * 4096;
        const unsigned ldst = lbase + w * 4096;
        const int wos = WS_WO + c3 * 65536 + w * 4096;
        const int fro = WS_FR + (((lane < 28 ? ((8 * c3 + w) >> 1) * 28 + lane : (lane < 42 ? 784 + c3 * 112 + 14 * w + (lane - 28) : 1568 + c3 * 32 + 4 * w + (lane & 3))) * 9363) >> 16) * 8;
        const LAS char* rd = lds + kh * 16384 + l16;
        const LAS char* rd2 = rd + 65536; asm volatile("" : "+v"(rd2));
        u32x4 q0, q1;
#pragma unroll 1
        for (int pass = 0; pass < 2; ++pass) {
            if (pass == 0) {
        if (w >= 6) wait_flags(rw, WS_F1 + (lane < 16 ? (((16 << (lane >> 3)) + head) * 2 + (w & 1)) * 128 + (lane & 7) * 8 : (head * 2 + (qb >> 3)) * 128 + (qb & 7) * 8), ep);
        BAR();
        STAMP(5);
            }
            q0 = ld_c_async(rw, l16, WS_Q + (head * 64 + qb * 4 + tt) * 2048); q1 = ld_c_async(rw, l16, WS_Q + (head * 64 + qb * 4 + tt) * 2048 + 1024);
            dma_c4(rw, l16, kvs, ldst);
            dma_c4(rw, l16, kvs + 16384, ldst + 32768);
            waitv<4>(); PIN(q0); PIN(q1); BAR();
            u32x2 frv;
            STAMP(6);
            float m;
            if (pass == 0) {
                f32x16 s = mfma(lds128(lds + l16), q0, zero); s = mfma(lds128(lds + 1024 + l16), q1, s);
                m = s[0];
#pragma unroll
                for (int r = 1; r < 16; ++r) m = fmaxf(m, s[r]);
            } else {
                m = -INFINITY;
#pragma unroll 1
                for (int t = 0; t < 64; ++t) {
                    f32x16 s = mfma(ld_c(rw, l16, WS_KV + (head * 64 + t) * 4096), q0, zero); s = mfma(ld_c(rw, l16 + 1024, WS_KV + (head * 64 + t) * 4096), q1, s);
#pragma unroll
                    for (int r = 0; r < 16; ++r) m = fmaxf(m, s[r]);
                }
            }
            {
                const float n = -max32(m); const unsigned t0 = pk2(n, 0.f) & 0xffffu; const float r1 = n - bflo(t0);
                const unsigned t1 = pk2(r1, 0.f) & 0xffffu; const unsigned t2 = pk2(r1 - bflo(t1), 0.f) & 0xffffu;
                if (h) { q1[2] = t0 | (t1 << 16); q1[3] = t2; }
            }
            o = zero;
            f32x16 sA, sB; u32x4 k0, k1, v0, v1;
            sA = mfma(lds128(rd), q0, zero); sA = mfma(lds128(rd + 1024), q1, sA);
            k0 = lds128(rd + 4096); k1 = lds128(rd + 4096 + 1024);
            v0 = lds128(rd + 2048); v1 = lds128(rd + 3072);
#define EX(P, n) P[n] = __builtin_amdgcn_exp2f(P[n])
#define E8(P, n) do { EX(P, n); EX(P, n + 1); EX(P, n + 2); EX(P, n + 3); EX(P, n + 4); EX(P, n + 5); EX(P, n + 6); EX(P, n + 7); PIN(P); SBAR(); } while (0)
#define PRIO(n) do { __builtin_amdgcn_s_setprio(n); SBAR(); } while (0)
#define TPTR(t) (((((t) >> 2) & 2) ? rd2 : rd) + (((t) >> 2) & 1) * 32768 + ((t) & 3) * 4096)
#define W1T(t) ((t) >= 6 && (t) < 30)
#define STEP(SC, SN, t) do { \
            constexpr bool sync_ = ((t) & 3) == 2 && (t) < 30, ld_ = (t) >= 2 && (t) < 30; \
            constexpr int nw_ = (t) <= 6 ? 4 : 8; \
            constexpr int st_ = (((t) - 2) >> 2) + 3, p_ = ((t) - 2) & 3; \
            if (sync_ && kh) { waitv<nw_>(); BAR(); } \
            PRIO(0); E8(SC, 0); PRIO(1); E8(SC, 8); PRIO(2); \
            if (sync_ && !kh) { waitv<nw_>(); BAR(); } \
            if ((t) == 6) { PIN(frv); if (!flag_ok(frv, ep)) wait_flags(rw, fro, ep); SBAR(); } \
            if ((t) < 31) { SN = mfma(k0, q0, zero); SBAR(); } \
            if ((t) < 2) { dma_co<((t) & 1) * 2048>(rw, l16, kvs + 32768, ldst + 65536); SBAR(); } \
            if ((t) == 2) { frv = ld_flag_async(rw, fro); SBAR(); } \
            if (W1T(t)) { a1[W1T(t) ? (t) - 6 : 0] = ld_c(rw, l16 + p_ * 1024, w1s + (st_ - 4) * 4096); SBAR(); } \
            if ((t) < 31) { SN = mfma(k1, q1, SN); SBAR(); } \
            if ((t) < 2) { dma_co<((t) & 1) * 2048 + 1024>(rw, l16, kvs + 32768, ldst + 65536); SBAR(); } \
            if (ld_) { dma_co<p_ * 1024>(rw, l16, st_ < 8 ? kvs + st_ * 16384 : wos + (st_ - 8) * 32768, ldst + (st_ & 3) * 32768); SBAR(); } \
            o = pv(v0, SC, 0, o); SBAR(); \
            if ((t) < 30) { k0 = lds128(TPTR((t) + 2)); k1 = lds128(TPTR((t) + 2) + 1024); SBAR(); } \
            if ((t) < 31) { v0 = lds128(TPTR((t) + 1) + 2048); SBAR(); } \
            o = pv(v1, SC, 1, o); SBAR(); if ((t) < 31) { v1 = lds128(TPTR((t) + 1) + 3072); SBAR(); } \
            } while (0)
#define STEP4(t) STEP(sA, sB, t); STEP(sB, sA, (t) + 1); STEP(sA, sB, (t) + 2); STEP(sB, sA, (t) + 3)
            CSTAMP(22); STAMP(23);
            STEP4(0); STEP4(4); STEP4(8); STEP4(12); STEP4(16); STEP4(20); STEP4(24); STEP4(28);
            __builtin_amdgcn_s_setprio(0);
            STAMP(7); CSTAMP(24);
            LAS f32x4* red = (LAS f32x4*)(lds + L_RED) + lane;
            LAS float* lsum = (LAS float*)(lds + L_FLAG + 64);
            float amax = fmaxf(fabsf(o[0]), fabsf(o[1]));
#pragma unroll
            for (int r = 2; r < 16; r += 2) amax = fmaxf(amax, fmaxf(fabsf(o[r]), fabsf(o[r + 1])));
            const bool bad = pass == 0 && __any(!(amax <= 1e30f));
            f32x8 mine = kh ? __builtin_shufflevector(o, o, 8, 9, 10, 11, 12, 13, 14, 15) : __builtin_shufflevector(o, o, 0, 1, 2, 3, 4, 5, 6, 7);
            const f32x8 give = kh ? __builtin_shufflevector(o, o, 0, 1, 2, 3, 4, 5, 6, 7) : __builtin_shufflevector(o, o, 8, 9, 10, 11, 12, 13, 14, 15);
            red[w * 128] = __builtin_shufflevector(give, give, 0, 1, 2, 3); red[w * 128 + 64] = __builtin_shufflevector(give, give, 4, 5, 6, 7);
            if (h) lsum[w * 32 + i] = o[12];
            if (lane == 0) ((LAS int*)(lds + L_FLAG))[w] = bad;
            BARRIER();
            const u32x4 f0 = *(const LAS u32x4*)(lds + L_FLAG), f1 = *(const LAS u32x4*)(lds + L_FLAG + 16);
            const f32x4 t0 = red[(w ^ 4) * 128], t1 = red[(w ^ 4) * 128 + 64];
            const float rl = __builtin_amdgcn_rcpf(lsum[tt * 32 + i] + lsum[(tt + 4) * 32 + i]);
            if (__builtin_amdgcn_readfirstlane(f0[0] | f0[1] | f0[2] | f0[3] | f1[0] | f1[1] | f1[2] | f1[3]) == 0) {
                const u32x4 p = {pk2((mine[0] + t0[0]) * rl, (mine[1] + t0[1]) * rl), pk2((mine[2] + t0[2]) * rl, (mine[3] + t0[3]) * rl),
                                 pk2((mine[4] + t1[0]) * rl, (mine[5] + t1[1]) * rl), pk2((mine[6] + t1[2]) * rl, (mine[7] + t1[3]) * rl)};
                st_c(p, rw, l16, WS_O + ((qb * 4 + tt) * 32 + head * 2 + kh) * 1024);
                waitv<0>();
                arrive(lds, 6, 8, rw, WS_F2 + (qb * 2 + (head >> 3)) * 128 + (head & 7) * 8, ep, lane);
                break;
            }
            waitv<0>(); BAR();
        }
    }
    STAMP(8);
    if (jj >= 28) {
        if (b == 255 && w == 0) {
#pragma unroll 1
            for (int n = 0; n < 4; ++n) wait_flags(rw, WS_FR + (n * 64 + lane) * 8, ep);
            if (lane == 0) st_flag(rw, WS_EPOCH, ep);
        }
        STAMP_OUT();
        return;
    }

    float cs1 = 0.f;
    u32x4 a2[28];
    const int w2s = WS_W2 + (2 * c3 * 112 + 14 * w) * 1024;
    {
        const int nt2 = w & 1, rt = (w >> 1) & 1;
        f32x16 acc;
        if (w < 4) {
            const u16* xp = x + (size_t)(r3 * 64 + rt * 32 + i) * D_ + c3 * 64 + 8 * h;
#pragma unroll
            for (int g = 0; g < 4; ++g) {
                const u32x4 xr = *(const u32x4*)(xp + 16 * g);
#pragma unroll
                for (int e = 0; e < 4; ++e) acc[4 * g + e] = nt2 ? bfhi(xr[e]) : bflo(xr[e]);
            }
        }
        auto ld1 = [&]() {
#pragma unroll
            for (int k = 24; k < 28; ++k) a1[k] = ld_c(rw, l16 + (k & 3) * 1024, w1s + (k >> 2) * 4096);
        };
        auto ld2 = [&]() {
#pragma unroll
            for (int k = 0; k < 8; ++k) a2[k] = ld_c(rw, l16, w2s + ((k & 1) * 112 + (k >> 1)) * 1024);
        };
        if (w < 4) { ld1(); ld2(); }
        SBAR();
        float cs2 = 0.f;
#pragma unroll
        for (int k = 0; k < 24; ++k) { cs1 = dot2(a1[k][0], ONES, cs1); cs2 = dot2(a1[k][1], ONES, cs2); cs1 = dot2(a1[k][2], ONES, cs1); cs2 = dot2(a1[k][3], ONES, cs2); }
        PIN(cs1); PIN(cs2); PIN(acc);
        if (w == 7) wait_flags(rw, WS_F2 + ((r3 >> 1) * 2 + ((lane >> 3) & 1)) * 128 + (lane & 7) * 8, ep);
        BAR();
        STAMP(9);
        dma_c4(rw, l16, WS_O + r3 * 65536 + w * 8192, lbase + 65536 + w * 8192);
        dma_c4(rw, l16, WS_O + r3 * 65536 + w * 8192 + 4096, lbase + 65536 + w * 8192 + 4096);
        waitv<0>(); BAR();
        STAMP(10);
        if (w < 4) {
            const LAS char* pa = lds + nt2 * 32768 + l16; const LAS char* pb = lds + 65536 + rt * 32768 + l16;
            u32x4 fa[4], fb[4];
#pragma unroll
            for (int k = 0; k < 4; ++k) { fa[k] = lds128(pa + k * 1024); fb[k] = lds128(pb + k * 1024); }
            SBAR();
#pragma unroll
            for (int k = 0; k < 32; ++k) {
                acc = mfma(fa[k & 3], fb[k & 3], acc); SBAR();
                if (k < 28) { fa[k & 3] = lds128(pa + (k + 4) * 1024); fb[k & 3] = lds128(pb + (k + 4) * 1024); SBAR(); }
            }
            const int rtile = r3 * 2 + rt, nt = 2 * c3 + nt2;
            const u32x4 p0 = pack8(acc, 0), p1 = pack8(acc, 1);
            st_c(p0, rw, l16, WS_H + (rtile * 28 + 2 * nt) * 1024); st_c(p1, rw, l16 + 1024, WS_H + (rtile * 28 + 2 * nt) * 1024);
            float s = 0.f, q = 0.f, s2 = 0.f, q2 = 0.f;
#pragma unroll
            for (int e = 0; e < 4; ++e) { s = dot2(p0[e], ONES, s); q = dot2(p0[e], p0[e], q); s2 = dot2(p1[e], ONES, s2); q2 = dot2(p1[e], p1[e], q2); }
            s = sum32(s + s2); q = sum32(q + q2);
            __builtin_amdgcn_raw_buffer_store_b32(__float_as_uint(h ? q : s), rw, lane * 4, WS_ST + (rtile * 14 + nt) * 256, 16);
            *(LAS u32x4*)(lds + L_HS + w * 2048 + l16) = p0; *(LAS u32x4*)(lds + L_HS + w * 2048 + 1024 + l16) = p1;
            STAMP(11);
            waitv<0>();
            arrive(lds, 0, 4, rw, WS_F3 + r3 * 128 + c3 * 8, ep, lane);
        } else { ld1(); ld2(); }
#pragma unroll
        for (int k = 24; k < 28; ++k) { cs1 = dot2(a1[k][0], ONES, cs1); cs2 = dot2(a1[k][1], ONES, cs2); cs1 = dot2(a1[k][2], ONES, cs1); cs2 = dot2(a1[k][3], ONES, cs2); }
        cs1 = sum32(cs1 + cs2);
    }
    {
        const int ft = 8 * c3 + w;
        const unsigned bias = b1[(ft >> 1) * 64 + 2 * i + (ft & 1)];
        unsigned ch, cl; split2(cs1, ch, cl);
        u32x4 ae = {h ? 0u : (ch | (cl << 16)), h ? 0u : (ch | (cl << 16)), h ? 0u : (bias | (bias << 16)), 0u};
        PIN(ae);
        if (w == 7) wait_flags(rw, WS_F3 + r3 * 128 + (lane < 7 ? lane : 0) * 8, ep);
        BAR();
        STAMP(12);
        dma_c4(rw, l16, WS_H + r3 * 57344 + w * 7168, lbase + w * 7168);
#pragma unroll
        for (int p = 4; p < 7; ++p) dma_c(rw, l16, WS_H + r3 * 57344 + w * 7168 + p * 1024, lbase + w * 7168 + p * 1024);
        if (w < 7) dma_c(rw, l16, WS_ST + r3 * 7168 + w * 1024, lbase + 57344 + w * 1024);
        waitv<0>(); BARRIER();
        STAMP(13);
        u32x4 fb[4];
#pragma unroll
        for (int k = 0; k < 4; ++k) fb[k] = lds128(lds + k * 1024 + l16);
#pragma unroll
        for (int rt = 0; rt < 2; ++rt) {
            f32x16 acc = zero;
            const LAS float* sp = (const LAS float*)(lds + 57344) + rt * 14 * 64 + lane;
            float st = 0.f, sv[14];
            u32x4 be; float g1, g2, sd;
            if (rt) __builtin_amdgcn_s_setprio(0); else __builtin_amdgcn_s_setprio(1);
            SBAR();
#pragma unroll
            for (int k = 0; k < 28; ++k) {
                acc = mfma(a1[k], fb[k & 3], acc); SBAR();
                if (rt * 28 + k + 4 < 56) fb[k & 3] = lds128(lds + (rt * 28 + k + 4) * 1024 + l16);
                if (k < 7) { sv[2 * k] = sp[2 * k * 64]; sv[2 * k + 1] = sp[(2 * k + 1) * 64]; }
                if (k >= 4 && k < 11) { st += sv[2 * (k - 4)] + sv[2 * (k - 4) + 1]; PIN(st); }
                if (k == 12) {
                    float sum, sq; both32(st, sum, sq);
                    const float mean = sum * (1.f / D_), var = fmaxf(sq * (1.f / D_) - mean * mean, 0.f);
                    const float rs1 = __builtin_amdgcn_rsqf(var + EPS);
                    const float rs = rs1 * __builtin_amdgcn_rsqf(var * rs1 * rs1 + EPS);
                    sd = __builtin_amdgcn_rcpf(rs); g1 = -2.3022081981f * rs; g2 = -0.1029432397f * rs * rs * rs;
                    PIN(g1); PIN(g2);
                    unsigned mh, ml, sh, sl; split2(-mean, mh, ml); split2(sd, sh, sl);
                    be = (u32x4){h ? 0u : (mh | (mh << 16)), h ? 0u : (ml | (ml << 16)), h ? 0u : (sh | (sl << 16)), 0u};
                    acc = mfma(ae, be, acc);
                }
                if (rt == 0 && k >= 8 && !(k & 1)) { const int q = 8 + (k - 8) / 2; a2[q] = ld_c(rw, l16, w2s + ((q & 1) * 112 + (q >> 1)) * 1024); }
                if (rt == 1 && k >= 3 && k < 23 && (k & 1)) { const int q = 18 + (k - 3) / 2; a2[q] = ld_c(rw, l16, w2s + ((q & 1) * 112 + (q >> 1)) * 1024); }
                if (rt == 1 && k == 23) { waitv<10>(); arrive(lds, 1, 8, rw, WS_F4 + (r3 * 2) * 128 + c3 * 8, ep, lane); }
                SBAR();
            }
#pragma unroll
            for (int r = 0; r < 16; ++r) {
                const float a = acc[r], e = __builtin_amdgcn_exp2f(a * __builtin_fmaf(a * a, g2, g1));
                acc[r] = a * __builtin_amdgcn_rcpf(__builtin_fmaf(e, sd, sd));
            }
            const int dst = WS_HID + ((r3 * 2 + rt) * 112 + 2 * ft) * 1024;
            st_c(pack8(acc, 0), rw, l16, dst); st_c(pack8(acc, 1), rw, l16 + 1024, dst);
            SBAR();
            if (rt == 0) STAMP(14);
        }
        STAMP(15);
        waitv<0>();
        arrive(lds, 2, 8, rw, WS_F4 + (r3 * 2 + 1) * 128 + c3 * 8, ep, lane);
    }
    {
        const int col = c3 * 64 + 2 * ((2 * w & 3) + 8 * (w >> 1) + 4 * h);
        const u32x2 bb = *(const u32x2*)(b2 + col);
        const int f4o = WS_F4 + r3 * 256 + (lane < 7 ? lane : 0) * 8;
        u32x4 bf[14];
        volatile LAS unsigned* seen = (volatile LAS unsigned*)(lds + L_CNT) + 5;
        if (w == 0) { wait_flags(rw, f4o, ep); *seen = 1u; }
        else {
#pragma unroll 1
            for (int spin = 0; spin < (1 << 24) && *seen == 0u; ++spin) __builtin_amdgcn_s_sleep(1);
        }
        asm volatile("" ::: "memory");
        STAMP(16);
        u32x2 fB;
#pragma unroll
        for (int k = 0; k < 14; ++k) { bf[k] = ld_c(rw, l16, WS_HID + (r3 * 224 + 14 * w + k) * 1024); if (k == FBPOS) fB = ld_flag(rw, f4o + 128); SBAR(); }
        if (!flag_ok(fB, ep)) wait_flags(rw, f4o + 128, ep);
        STAMP(17);
#pragma unroll
        for (int rt = 0; rt < 2; ++rt) {
            f32x16 acc0 = zero, acc1 = zero;
#pragma unroll
            for (int k = 0; k < 14; ++k) {
                acc0 = mfma(a2[2 * k], bf[k], acc0); acc1 = mfma(a2[2 * k + 1], bf[k], acc1); SBAR();
                if (rt == 0) { bf[k] = ld_c(rw, l16, WS_HID + (r3 * 224 + 112 + 14 * w + k) * 1024); SBAR(); }
            }
            if (rt == 0) STAMP(18); else STAMP(20);
            LAS f32x4* red = (LAS f32x4*)(lds + (rt ? 0 : 65536)) + lane;
#pragma unroll
            for (int p = 0; p < 8; ++p) red[(p * 8 + w) * 64] = (f32x4){acc0[2 * p], acc1[2 * p], acc0[2 * p + 1], acc1[2 * p + 1]};
            SBAR();
            if (rt == 0) STAMP(19);
        }
        BARRIER();
#pragma unroll
        for (int rt = 0; rt < 2; ++rt) {
            const LAS f32x4* red = (const LAS f32x4*)(lds + (rt ? 0 : 65536)) + lane;
            f32x4 r = red[(w * 8) * 64];
#pragma unroll
            for (int q = 1; q < 8; ++q) r += red[(w * 8 + q) * 64];
            const LAS char* hp = lds + L_HS + rt * 4096 + (w >> 2) * 1024 + l16 + (w & 3) * 4;
            const unsigned h0 = *(const LAS unsigned*)hp, h1 = *(const LAS unsigned*)(hp + 2048);
            const u32x2 res = {pk2(r[0] + bflo(bb[0]) + bflo(h0), r[1] + bfhi(bb[0]) + bflo(h1)), pk2(r[2] + bflo(bb[1]) + bfhi(h0), r[3] + bfhi(bb[1]) + bfhi(h1))};
            *(u32x2*)(out + (size_t)(r3 * 64 + rt * 32 + i) * D_ + col) = res;
        }
    }
    STAMP(21);
    STAMP_OUT();
}

extern "C" void kernel_launch(void* const* d_in, const int* in_sizes, int n_in, void* d_out, int out_size, void* d_ws, size_t ws_size, hipStream_t stream) {
    (void)hipFuncSetAttribute((const void*)fused_block, hipFuncAttributeMaxDynamicSharedMemorySize, LDS_BYTES);
    fused_block<<<256, 512, LDS_BYTES, stream>>>((const u16*)d_in[0], (const u16*)d_in[1], (const u16*)d_in[2], (const u16*)d_in[4], (const u16*)d_in[5],
                                                 (const u16*)d_in[6], (const u16*)d_in[7], (u16*)d_out, (char*)d_ws);
}
```

```cpp
#define DIAGK 0
#include <hip/hip_runtime.h>
#include <stdint.h>

typedef unsigned short u16;
typedef unsigned long long u64;
typedef __bf16 bf16x2 __attribute__((ext_vector_type(2)));
typedef __bf16 bf16x8 __attribute__((ext_vector_type(8)));
typedef __bf16 bf16x16 __attribute__((ext_vector_type(16)));
typedef float f32x2 __attribute__((ext_vector_type(2)));
typedef float f32x4 __attribute__((ext_vector_type(4)));
typedef float f32x8 __attribute__((ext_vector_type(8)));
typedef float f32x16 __attribute__((ext_vector_type(16)));
typedef unsigned u32x2 __attribute__((ext_vector_type(2)));
typedef unsigned u32x4 __attribute__((ext_vector_type(4)));
typedef short s16x4 __attribute__((ext_vector_type(4)));
typedef __amdgpu_buffer_rsrc_t rsrc_t;
#define LAS __attribute__((address_space(3)))
#define DI __device__ __forceinline__
#define SBAR() __builtin_amdgcn_sched_barrier(0)
#define PIN(x) asm volatile("" : "+v"(x))

constexpr int CODE_BYTES = 28672, CODE_BACK = 0;
#define FBPOS 13
#define W1MODE 1
constexpr int D_ = 448, F_ = 1792;
constexpr float EPS = 1e-5f;
constexpr int WS_EPOCH = 0;
constexpr int WS_F1 = 1024;
constexpr int WS_FR = WS_F1 + 96 * 128;
constexpr int WS_F2 = WS_FR + 2048;
constexpr int WS_F3 = WS_F2 + 32 * 128;
constexpr int WS_F4 = WS_F3 + 32 * 128;
constexpr int WS_ST = 32768;
constexpr int WS_Q = WS_ST + 64 * 14 * 256;
constexpr int WS_KV = WS_Q + 16 * 64 * 2048;
constexpr int WS_O = WS_KV + 16 * 64 * 4096;
constexpr int WS_H = WS_O + 64 * 32 * 1024;
constexpr int WS_W1 = WS_H + 64 * 28 * 1024;
constexpr int WS_W2 = WS_W1 + 56 * 28 * 1024;
constexpr int WS_WO = WS_W2 + 14 * 112 * 1024;
constexpr int WS_HID = WS_WO + 14 * 32 * 1024;
constexpr int WS_END = WS_HID + 64 * 112 * 1024;
static_assert(WS_F4 + 64 * 128 <= WS_ST, "flags overlap");

constexpr int XCH = 16384, WCH = 12288, CHB = XCH + WCH;
constexpr int L_MISC = 5 * CHB;
constexpr int L_CS = L_MISC, L_XE = L_MISC + 3072, L_CW = L_XE + 4096, L_RT = L_CW + 768;
constexpr int L_VT = 2 * CHB;
constexpr int L_RED = 131072;
constexpr int L_FLAG = L_RED + 16384;
constexpr int L_HS = L_RED + 16384 + 64 + 1024;
constexpr int L_CNT = 163840 - 1024 - 64;
constexpr int L_DUMP = 163840 - 1024;
constexpr int LDS_BYTES = 163840;

#define STAMP_DECL unsigned long long tref = 0
#define STAMP(i) do { if ((i) == DIAGK) asm volatile("s_memrealtime %0\n\ts_waitcnt lgkmcnt(0)" : "=s"(tref) :: "memory"); } while (0)
#define CSTAMP(i)
#define STAMP_OUT() do { if (tref) while (__builtin_amdgcn_s_memrealtime() - tref < 6000ull) __builtin_amdgcn_s_sleep(1); } while (0)

DI f32x16 mfma(u32x4 a, u32x4 b, f32x16 c) {
    return __builtin_amdgcn_mfma_f32_32x32x16_bf16(__builtin_bit_cast(bf16x8, a), __builtin_bit_cast(bf16x8, b), c, 0, 0, 0);
}
DI f32x4 mfma16(u32x4 a, u32x4 b, f32x4 c) {
    return __builtin_amdgcn_mfma_f32_16x16x32_bf16(__builtin_bit_cast(bf16x8, a), __builtin_bit_cast(bf16x8, b), c, 0, 0, 0);
}
DI f32x4 mfma4(unsigned a0, unsigned a1, unsigned b0, unsigned b1, f32x4 c) {
    return __builtin_amdgcn_mfma_f32_4x4x4bf16_1k(__builtin_bit_cast(s16x4, (u32x2){a0, a1}), __builtin_bit_cast(s16x4, (u32x2){b0, b1}), c, 0, 0, 0);
}
DI f32x16 pv(u32x4 v, const f32x16& p, int s, f32x16 o) {
    f32x8 ph = {p[8 * s], p[8 * s + 1], p[8 * s + 2], p[8 * s + 3], p[8 * s + 4], p[8 * s + 5], p[8 * s + 6], p[8 * s + 7]};
    return __builtin_amdgcn_smfmac_f32_32x32x32_bf16(__builtin_bit_cast(bf16x8, v), __builtin_bit_cast(bf16x16, ph), o, 0xDDDD, 0, 0);
}
DI unsigned pk2(float lo, float hi) { f32x2 v = {lo, hi}; return __builtin_bit_cast(unsigned, __builtin_convertvector(v, bf16x2)); }
DI float bflo(unsigned u) { return __uint_as_float(u << 16); }
DI float bfhi(unsigned u) { return __uint_as_float(u & 0xffff0000u); }
DI float dot2(unsigned a, unsigned b, float c) { return __builtin_amdgcn_fdot2_f32_bf16(__builtin_bit_cast(bf16x2, a), __builtin_bit_cast(bf16x2, b), c, false); }
DI void both32(float v, float& lo, float& hi) {
    auto r = __builtin_amdgcn_permlane32_swap(__float_as_uint(v), __float_as_uint(v), false, false);
    lo = __uint_as_float(r[0]); hi = __uint_as_float(r[1]);
}
DI float sum32(float v) { float a, b; both32(v, a, b); return a + b; }
DI float max32(float v) { float a, b; both32(v, a, b); return fmaxf(a, b); }
DI u32x4 lds128(const LAS char* p) { return *(const LAS u32x4*)p; }
DI u32x2 ldstr(const LAS char* p) { return __builtin_bit_cast(u32x2, __builtin_amdgcn_ds_read_tr16_b64_v4i16((LAS s16x4*)p)); }
DI void split2(float v, unsigned& hi, unsigned& lo) { hi = pk2(v, 0.f) & 0xffffu; lo = pk2(v - bflo(hi), 0.f) & 0xffffu; }
DI u32x4 pack8(const f32x16& a, int s) {
    u32x4 r;
    r[0] = pk2(a[8 * s + 0], a[8 * s + 1]); r[1] = pk2(a[8 * s + 2], a[8 * s + 3]);
    r[2] = pk2(a[8 * s + 4], a[8 * s + 5]); r[3] = pk2(a[8 * s + 6], a[8 * s + 7]);
    return r;
}
DI void dma(rsrc_t rs, int voff, int soff, unsigned lds_addr) {
    asm volatile("s_mov_b32 m0, %0\n\ts_nop 3\n\tbuffer_load_dwordx4 %1, %2, %3 offen lds" :: "s"(lds_addr), "v"(voff), "s"(rs), "s"(soff) : "memory");
}
DI void dma_c(rsrc_t rs, int voff, int soff, unsigned lds_addr) {
    asm volatile("s_mov_b32 m0, %0\n\ts_nop 3\n\tbuffer_load_dwordx4 %1, %2, %3 offen sc1 lds" :: "s"(lds_addr), "v"(voff), "s"(rs), "s"(soff) : "memory");
}
DI void dma_c4(rsrc_t rs, int voff, int soff, unsigned lds_addr) {
    asm volatile("s_mov_b32 m0, %0\n\ts_nop 3\n\tbuffer_load_dwordx4 %1, %2, %3 offen sc1 lds\n\tbuffer_load_dwordx4 %1, %2, %3 offen offset:1024 sc1 lds\n\t"
                 "buffer_load_dwordx4 %1, %2, %3 offen offset:2048 sc1 lds\n\tbuffer_load_dwordx4 %1, %2, %3 offen offset:3072 sc1 lds"
                 :: "s"(lds_addr), "v"(voff), "s"(rs), "s"(soff) : "memory");
}
DI u32x4 ld_c(rsrc_t rs, int voff, int soff) { return __builtin_amdgcn_raw_buffer_load_b128(rs, voff, soff, 16); }
DI void st_c(u32x4 v, rsrc_t rs, int voff, int soff) { __builtin_amdgcn_raw_buffer_store_b128(v, rs, voff, soff, 16); }
DI u32x2 ld_flag(rsrc_t rs, int voff) { asm volatile("" ::: "memory"); return __builtin_amdgcn_raw_buffer_load_b64(rs, voff, 0, 16); }
DI void st_flag(rsrc_t rs, int voff, u32x2 e) { __builtin_amdgcn_raw_buffer_store_b64(e, rs, voff, 0, 16); }
template <int N> DI void waitv() {
    asm volatile("s_waitcnt vmcnt(%0)" :: "n"(N) : "memory");
    if (N == 0) __builtin_amdgcn_s_waitcnt(0x0F70);
}
#define BAR() asm volatile("s_barrier" ::: "memory")
#define BARRIER() asm volatile("s_waitcnt lgkmcnt(0)\n\ts_barrier" ::: "memory")
DI bool flag_ok(u32x2 f, u32x2 e) { return __all(f[0] == e[0] && f[1] == e[1]); }
DI void wait_flags(rsrc_t rs, int voff, u32x2 e) {
#pragma unroll 1
    for (int spin = 0; spin < (1 << 18); ++spin) {
        if (flag_ok(ld_flag(rs, voff), e)) break;
        __builtin_amdgcn_s_sleep(1);
    }
    asm volatile("" ::: "memory");
}
DI void arrive(LAS char* lds, int cnt, int n, rsrc_t rs, int foff, u32x2 e, int lane) {
    if (lane == 0) {
        const unsigned old = __hip_atomic_fetch_add((LAS unsigned*)(lds + L_CNT) + cnt, 1u, __ATOMIC_RELAXED, __HIP_MEMORY_SCOPE_WORKGROUP);
        if (old == (unsigned)(n - 1)) st_flag(rs, foff, e);
    }
}
template <int OFF> DI void dma_co(rsrc_t rs, int voff, int soff, unsigned lds_addr) {
    asm volatile("s_mov_b32 m0, %0\n\ts_nop 3\n\tbuffer_load_dwordx4 %1, %2, %3 offen offset:%4 sc1 lds" :: "s"(lds_addr), "v"(voff), "s"(rs), "s"(soff), "n"(OFF) : "memory");
}
DI u32x4 ld_c_async(rsrc_t rs, int voff, int soff) { u32x4 r; asm volatile("s_nop 4\n\tbuffer_load_dwordx4 %0, %1, %2, %3 offen sc1" : "=v"(r) : "v"(voff), "s"(rs), "s"(soff) : "memory"); return r; }
DI u32x2 ld_flag_async(rsrc_t rs, int voff) { u32x2 r; asm volatile("buffer_load_dwordx2 %0, %1, %2, 0 offen sc1" : "=v"(r) : "v"(voff), "s"(rs) : "memory"); return r; }
DI float gelu_tanh(float x) {
    const float t = __builtin_fmaf(x * x, -0.1029432397f, -2.3022081981f);
    const float e = __builtin_amdgcn_exp2f(x * t);
    return x * __builtin_amdgcn_rcpf(1.f + e);
}

__global__ __launch_bounds__(512, 2) void fused_block(const u16* __restrict__ x, const u16* __restrict__ wqkv, const u16* __restrict__ wo,
                                                       const u16* __restrict__ w1, const u16* __restrict__ b1, const u16* __restrict__ w2,
                                                       const u16* __restrict__ b2, u16* __restrict__ out, char* __restrict__ ws) {
    extern __shared__ __attribute__((aligned(16))) char smem[];
    LAS char* lds = (LAS char*)smem;
    const unsigned lbase = (unsigned)(uintptr_t)lds;
    const int lane = threadIdx.x & 63, w = __builtin_amdgcn_readfirstlane(threadIdx.x >> 6);
    const int i = lane & 31, h = lane >> 5, l16 = lane * 16;
    const int b = blockIdx.x, xcd = b & 7, jj = b >> 3;
    STAMP_DECL; STAMP(0);
    const int rb = 4 * (xcd >> 1) + (jj & 3), ct = 8 * (xcd & 1) + (jj >> 2);
    const int tt = w & 3;
        const rsrc_t rx = __builtin_amdgcn_make_buffer_rsrc((void*)x, 0, 2048 * 896, 0x00020000);
        const rsrc_t rq = __builtin_amdgcn_make_buffer_rsrc((void*)wqkv, 0, 448 * 2688, 0x00020000);
        const int vx0 = (lane >> 3) * 896 + (((lane & 7) ^ (lane >> 4)) << 4), vx1 = vx0 ^ 64;
        int vw[3]; u64 mk[3];
#pragma unroll
        for (int p = 0; p < 3; ++p) { const int q = (3 * tt + p) * 64 + lane, row = (q * 43691) >> 19; vw[p] = row * (2688 - 192) + q * 16; mk[p] = __ballot(q - row * 12 != 11); }
        int cg = (ct + 2 * rb) % 7;
        const int sx0 = (rb * 128 + 32 * tt) * 896, sw0 = (168 * ct) & ~15;
        unsigned ldn = lbase;
        auto issue1 = [&](int p) {
            if (p < 4) dma(rx, (p & 1) ? vx1 : vx0, sx0 + p * 7168 + cg * 128, ldn + (4 * tt + p) * 1024);
            else
                asm volatile("s_mov_b32 m0, %0\n\ts_mov_b64 exec, %4\n\ts_nop 2\n\tbuffer_load_dwordx4 %1, %2, %3 offen lds\n\ts_mov_b64 exec, -1"
                             :: "s"(ldn + XCH + (3 * tt + p - 4) * 1024), "v"(vw[p - 4]), "s"(rq), "s"(sw0 + cg * (64 * 2688)), "s"(mk[p - 4]) : "memory");
        };
    if (w >= 4) {
#pragma unroll 1
        for (int c = 0; c < 2; ++c) {
#pragma unroll
            for (int p = 0; p < 7; ++p) issue1(p);
            cg = cg == 6 ? 0 : cg + 1; ldn += CHB;
        }
    }
    STAMP(28);
    SBAR();
    const rsrc_t rw = __builtin_amdgcn_make_buffer_rsrc(ws, 0, WS_END, 0x00020000);
    const f32x16 zero = {};
    const unsigned ONES = 0x3f803f80u;
    u32x2 ep;
    u32x4 rte[2], rto[2]; int rdst[2], rts[2];
    if (w >= 4) {
        __builtin_amdgcn_s_setprio(3);
        const int r16 = lane & 15, g16 = lane >> 4;
        const LAS char* xp = lds + (32 * tt + r16) * 128;
        const LAS char* wp = lds + XCH + (32 * (tt >> 1) + 8 * g16 + (r16 >> 2)) * 192 + ((168 * ct) & 15) + ((tt & 1) * 16 + (lane & 3) * 4) * 2;
        const u32x4 ones4 = {ONES, ONES, ONES, ONES};
        f32x4 sq[2] = {{}, {}}, cs[3] = {{}, {}, {}};
        u32x4 xf[4], wf[3];
        int sl = 0;
        auto rd = [&](int p) {
            if (p < 4) xf[p] = lds128(xp + sl + (p & 1) * 2048 + ((((p >> 1) * 4 + g16) ^ (r16 >> 1)) << 4));
            else { const u32x2 t0 = ldstr(wp + sl + (p - 4) * 56), t1 = ldstr(wp + sl + (p - 4) * 56 + 768); wf[p - 4] = (u32x4){t0[0], t0[1], t1[0], t1[1]}; }
            SBAR();
        };
        auto acc_ = [&](int p) {
            if (p < 4) sq[p & 1] = mfma16(xf[p], xf[p], sq[p & 1]);
            else cs[p - 4] = mfma16(ones4, wf[p - 4], cs[p - 4]);
            SBAR();
        };
        waitv<7>(); BAR();
        STAMP(1);
#pragma unroll
        for (int p = 0; p < 7; ++p) { issue1(p); rd(p); }
        cg = cg == 6 ? 0 : cg + 1; ldn += CHB;
#pragma unroll
        for (int p = 0; p < 7; ++p) issue1(p);
        cg = cg == 6 ? 0 : cg + 1; ldn += CHB;
#pragma unroll 1
        for (int c = 4; c < 7; ++c) {
            waitv<14>(); BAR();
            sl += CHB;
#pragma unroll
            for (int p = 0; p < 7; ++p) { issue1(p); acc_(p); rd(p); }
            cg = cg == 6 ? 0 : cg + 1; ldn = ldn == lbase + 4 * CHB ? lbase : ldn + CHB;
        }
        unsigned rv[2][8];
        STAMP(25);
#pragma unroll
        for (int n = 0; n < 3; ++n) {
            rsrc_t rr; int rst, rs0, v0, v1;
            if (n < 2) {
                const u32x4 pa = lds128(lds + L_RT + (tt * 2 + n) * 32), pb = lds128(lds + L_RT + (tt * 2 + n) * 32 + 16);
                const u64 src = (u64)(unsigned)__builtin_amdgcn_readfirstlane(pa[0]) | ((u64)(unsigned)__builtin_amdgcn_readfirstlane(pa[1]) << 32);
                rr = __builtin_amdgcn_make_buffer_rsrc((void*)src, 0, __builtin_amdgcn_readfirstlane(pa[2]), 0x00020000);
                rst = __builtin_amdgcn_readfirstlane(pa[3]); rs0 = __builtin_amdgcn_readfirstlane(pb[0]);
                rdst[n] = __builtin_amdgcn_readfirstlane(pb[1]); rts[n] = __builtin_amdgcn_readfirstlane(pb[2]);
                v0 = 4 * h * rst + 4 * i; v1 = v0 | (h ? pb[3] : 0);
            }
            if (n == 0) waitv<14>(); else if (n == 1) waitv<15>(); else waitv<16>();
            if (n == 2) STAMP(26);
            BAR();
            if (n == 2) STAMP(27);
            sl = sl == 4 * CHB ? 0 : sl + CHB;
#pragma unroll
            for (int p = 0; p < 8; ++p) {
                if (p < 7) { acc_(p); rd(p); }
                if (n < 2) { rv[n][p] = __builtin_amdgcn_raw_buffer_load_b32(rr, p < 4 ? v0 : v1, rs0 + rst * (8 * (p >> 2) + (p & 3)), 0); SBAR(); }
            }
        }
#pragma unroll
        for (int p = 0; p < 7; ++p) acc_(p);
        LAS float* csb = (LAS float*)(lds + L_CS);
        if (lane < 16) {
#pragma unroll
            for (int s = 0; s < 3; ++s) csb[(s * 2 + (tt >> 1)) * 32 + (tt & 1) * 16 + lane] = cs[s][0];
        }
#pragma unroll
        for (int tb = 0; tb < 2; ++tb) {
            const float q01 = (lane & 1) ? sq[tb][1] : sq[tb][0], q23 = (lane & 1) ? sq[tb][3] : sq[tb][2];
            if (g16 == (r16 >> 2)) ((LAS float*)(lds + L_XE))[tt * 32 + tb * 16 + r16] = (lane & 2) ? q23 : q01;
        }
        STAMP(2);
        BARRIER();
        __builtin_amdgcn_s_setprio(0);
        ep = *(const LAS u32x2*)(lds + L_CNT + 32);
#pragma unroll
        for (int n = 0; n < 2; ++n) {
#pragma unroll
            for (int j = 0; j < 4; ++j) { rte[n][j] = __builtin_amdgcn_perm(rv[n][2 * j + 1], rv[n][2 * j], 0x05040100u); rto[n][j] = __builtin_amdgcn_perm(rv[n][2 * j + 1], rv[n][2 * j], 0x07060302u); }
            if (n == 0 || tt < 3) { st_c(rte[n], rw, l16, rdst[n]); st_c(rto[n], rw, l16, rdst[n] + rts[n]); }
        }
        STAMP(3);
        waitv<0>();
        STAMP(4);
        arrive(lds, 3, 4, rw, WS_FR + b * 8, ep, lane);
    } else {
        if (w == 0) {
            u64 pc; asm volatile("s_getpc_b64 %0" : "=s"(pc));
            const rsrc_t rc = __builtin_amdgcn_make_buffer_rsrc((void*)((pc & ~255ull) - CODE_BACK), 0, CODE_BYTES, 0x00020000);
            dma(rc, l16, jj * 1024, lbase + L_DUMP);
            if (lane < 8) ((LAS unsigned*)(lds + L_CNT))[lane] = 0u;
        }
        if (w == 3) {
            const u64 e = __builtin_bit_cast(u64, __builtin_amdgcn_raw_buffer_load_b64(rw, 0, WS_EPOCH, 16)) + 1;
            if (lane == 0) *(LAS u64*)(lds + L_CNT + 32) = e;
        }
        if (lane < 2) {
            const int u = 7 * b + (lane && tt < 3 ? tt + 4 : tt);
            const bool is1 = u < 784, is2 = u < 1568;
            const int q = is1 ? u : (is2 ? u - 784 : u - 1568), nks = is1 ? 28 : (is2 ? 112 : 32);
            const int win = is1 ? q / 28 : (is2 ? q / 112 : q >> 5), ks = q - win * nks;
            const int r0 = is2 ? 64 * (ks >> 2) + ((ks >> 1) & 1) + 32 * (ks & 1) : 28 * (ks >> 1) + 16 * (ks & 1);
            const u64 src = (u64)(is1 ? w1 : (is2 ? w2 : wo));
            LAS u32x4* pr = (LAS u32x4*)(lds + L_RT + (tt * 2 + lane) * 32);
            pr[0] = (u32x4){(unsigned)src, (unsigned)(src >> 32), (unsigned)(is2 ? 2 * D_ * F_ : 2 * D_ * D_), (unsigned)(is1 ? 4 * F_ : (is2 ? 4 * D_ : 2 * D_))};
            pr[1] = (u32x4){(unsigned)(r0 * (is1 ? 2 * F_ : 2 * D_) + win * 128), (unsigned)((is1 ? WS_W1 : (is2 ? WS_W2 : WS_WO)) + (q + win * nks) * 1024),
                            (unsigned)(nks * 1024), (!is2 && (ks & 1)) ? 0x40000000u : 0u};
        }
        const LAS char* xp[4];
#pragma unroll
        for (int ks = 0; ks < 4; ++ks) xp[ks] = lds + (32 * tt + i) * 128 + (((2 * ks + h) ^ ((i >> 1) & 7)) << 4);
        const int wcol = ((168 * ct) & 15) + (((lane >> 4) & 1) * 16 + (lane & 3) * 4) * 2;
        const LAS char* wp = lds + XCH + (8 * h + ((lane & 15) >> 2)) * 192 + wcol;
        const LAS char* wp2 = wp + ((lane & 19) == 19 ? 176 - wcol : 112);
        *(LAS u32x2*)(lds + tt * CHB + XCH + lane * 192 + 176) = (u32x2){ONES, ONES};
        if (w == 0) *(LAS u32x2*)(lds + 4 * CHB + XCH + lane * 192 + 176) = (u32x2){ONES, ONES};
        unsigned scl[3], pd2[3], pd3[3]; int dst[3];
#pragma unroll
        for (int s = 0; s < 3; ++s) {
            const int sec = 3 * ct + s, tl = (sec & 15) * 64 + rb * 4 + tt;
            scl[s] = sec < 16 ? __builtin_bit_cast(unsigned, 0.18898223650461361f * 1.4426950408889634f) : 0x3f800000u;
            pd2[s] = sec < 16 ? 0u : (sec < 32 ? ONES : 0x3f80u); pd3[s] = (sec >= 16 && sec < 32) ? 0x3f80u : 0u;
            dst[s] = sec < 16 ? WS_Q + tl * 2048 : WS_KV + tl * 4096 + (sec >= 32 ? 2048 : 0);
            asm volatile("" : "+s"(scl[s]), "+s"(pd2[s]), "+s"(pd3[s]), "+s"(dst[s]));
        }
        LAS char* vtw = lds + L_VT + w * 6912 + i * 72 + h * 8;
        const LAS char* vtr = lds + L_VT + w * 6912 + (8 * h + ((lane & 15) >> 2)) * 72 + ((lane >> 4) & 1) * 32 + (lane & 3) * 8;
        int f1o = WS_F1 + ((3 * ct + lane) * 2 + (rb >> 3)) * 128 + (rb & 7) * 8;
        PIN(vtw); PIN(vtr); PIN(f1o);
        f32x16 acc[3] = {zero, zero, zero};
        PIN(acc[0]); PIN(acc[1]); PIN(acc[2]);
        u32x4 xf[4], wf[4][3];
        auto rdw = [&](int ks, int s, const LAS char* p) {
            const u32x2 t0 = ldstr(p + ks * 3072 + (s & 1) * 56), t1 = ldstr(p + ks * 3072 + (s & 1) * 56 + 768);
            wf[ks][s] = (u32x4){t0[0], t0[1], t1[0], t1[1]};
        };
        BARRIER();
        STAMP(1);
#pragma unroll
        for (int ks = 0; ks < 4; ++ks) { xf[ks] = lds128(xp[ks]); rdw(ks, 0, wp); rdw(ks, 1, wp); rdw(ks, 2, wp2); SBAR(); }
        int sl = CHB;
#pragma unroll 1
        for (int c = 1; c < 7; ++c) {
            BAR();
            const LAS char* p = wp + sl; const LAS char* p2 = wp2 + sl;
#pragma unroll
            for (int ks = 0; ks < 4; ++ks)
#pragma unroll
                for (int s = 0; s < 3; ++s) {
                    acc[s] = mfma(wf[ks][s], xf[ks], acc[s]); SBAR();
                    if (s == 2) xf[ks] = lds128(xp[ks] + sl);
                    rdw(ks, s, s == 2 ? p2 : p); SBAR();
                }
            sl = sl == 4 * CHB ? 0 : sl + CHB;
        }
#pragma unroll
        for (int ks = 0; ks < 4; ++ks) { acc[2] = mfma(wf[ks][2], xf[ks], acc[2]); SBAR(); }
        acc[0] = mfma(wf[0][0], xf[0], acc[0]); SBAR();
        BAR();
        ep = *(const LAS u32x2*)(lds + L_CNT + 32);
        const float sqv = ((const LAS float*)(lds + L_XE))[tt * 32 + i];
        float cv[6];
#pragma unroll
        for (int s = 0; s < 6; ++s) cv[s] = ((const LAS float*)(lds + L_CS))[s * 32 + i];
        SBAR();
        acc[1] = mfma(wf[0][1], xf[0], acc[1]); SBAR();
        float lo_, sxv; both32(acc[2][12], lo_, sxv);
        const float mean = sxv * (1.f / D_);
        unsigned mh, ml; split2(-mean, mh, ml);
        u32x4 xe = {h ? 0u : (mh | (mh << 16)), h ? 0u : (ml | (ml << 16)), 0u, 0u};
        PIN(xe); SBAR();
        acc[0] = mfma(wf[1][0], xf[1], acc[0]); SBAR();
        float rstd = __builtin_amdgcn_rsqf(fmaxf(sqv * (1.f / D_) - mean * mean, 0.f) + EPS);
        PIN(rstd); SBAR();
        u32x4 we[3];
#pragma unroll
        for (int s = 0; s < 3; ++s) {
            if (s == 0) acc[1] = mfma(wf[1][1], xf[1], acc[1]); else acc[s - 1] = mfma(wf[2][s - 1], xf[2], acc[s - 1]);
            SBAR();
            unsigned ch, cl; split2(cv[2 * s] + cv[2 * s + 1], ch, cl);
            const unsigned cw = h ? 0u : (ch | (cl << 16));
            we[s] = (u32x4){cw, cw, 0u, 0u};
            PIN(we[s]); SBAR();
        }
        acc[0] = mfma(wf[3][0], xf[3], acc[0]); SBAR();
        acc[0] = mfma(we[0], xe, acc[0]); SBAR();
        acc[1] = mfma(wf[3][1], xf[3], acc[1]); SBAR();
        acc[1] = mfma(we[1], xe, acc[1]); SBAR();
        acc[2] = mfma(we[2], xe, acc[2]); SBAR();
        STAMP(2);
        u32x4 p0[3], p1[3];
#pragma unroll
        for (int s = 0; s < 3; ++s) {
            const f32x16 v = acc[s] * (rstd * __uint_as_float(scl[s]));
            p0[s] = pack8(v, 0); p1[s] = pack8(v, 1);
            if (h) { p1[s][2] = pd2[s]; p1[s][3] = pd3[s]; }
            if (3 * ct + s >= 32) {
                LAS u32x2* wq = (LAS u32x2*)(vtw + s * 2304);
                wq[0] = (u32x2){p0[s][0], p0[s][1]}; wq[2] = (u32x2){p0[s][2], p0[s][3]}; wq[4] = (u32x2){p1[s][0], p1[s][1]}; wq[6] = (u32x2){p1[s][2], p1[s][3]};
                const u32x2 t0 = ldstr(vtr + s * 2304), t1 = ldstr(vtr + s * 2304 + 288), t2 = ldstr(vtr + s * 2304 + 1152), t3 = ldstr(vtr + s * 2304 + 1440);
                p0[s] = (u32x4){t0[0], t0[1], t1[0], t1[1]}; p1[s] = (u32x4){t2[0], t2[1], t3[0], t3[1]};
            }
            if (s) { st_c(p0[s - 1], rw, l16, dst[s - 1]); st_c(p1[s - 1], rw, l16 + 1024, dst[s - 1]); }
        }
        st_c(p0[2], rw, l16, dst[2]); st_c(p1[2], rw, l16 + 1024, dst[2]);
        STAMP(3);
        waitv<0>();
        STAMP(4);
        unsigned old = 0;
        if (lane == 0) old = __hip_atomic_fetch_add((LAS unsigned*)(lds + L_CNT) + 4, 1u, __ATOMIC_RELAXED, __HIP_MEMORY_SCOPE_WORKGROUP);
        if (__builtin_amdgcn_readfirstlane(old) == 3 && lane < 3) st_flag(rw, f1o, ep);
    }
    ep[0] = __builtin_amdgcn_readfirstlane(ep[0]); ep[1] = __builtin_amdgcn_readfirstlane(ep[1]);

    const int head = 2 * xcd + (jj & 1), qb = jj >> 1, kh = w >> 2;
    const int r3 = 4 * xcd + (jj & 3), c3 = jj >= 28 ? 6 : (jj >> 2);
    u32x4 a1[28];
    const int w1s = WS_W1 + (8 * c3 + w) * 28 * 1024;
    f32x16 o;
    {
        const int kvs = WS_KV + head * 64 * 4096 + (kh * 32 + tt) * 4096;
        const unsigned ldst = lbase + w * 4096;
        const int wos = WS_WO + c3 * 65536 + w * 4096;
        const int fro = WS_FR + (((lane < 28 ? ((8 * c3 + w) >> 1) * 28 + lane : (lane < 42 ? 784 + c3 * 112 + 14 * w + (lane - 28) : 1568 + c3 * 32 + 4 * w + (lane & 3))) * 9363) >> 16) * 8;
        const LAS char* rd = lds + kh * 16384 + l16;
        const LAS char* rd2 = rd + 65536; asm volatile("" : "+v"(rd2));
        u32x4 q0, q1;
#pragma unroll 1
        for (int pass = 0; pass < 2; ++pass) {
            if (pass == 0) {
        if (w >= 6) wait_flags(rw, WS_F1 + (lane < 16 ? (((16 << (lane >> 3)) + head) * 2 + (w & 1)) * 128 + (lane & 7) * 8 : (head * 2 + (qb >> 3)) * 128 + (qb & 7) * 8), ep);
        BAR();
        STAMP(5);
            }
            q0 = ld_c_async(rw, l16, WS_Q + (head * 64 + qb * 4 + tt) * 2048); q1 = ld_c_async(rw, l16, WS_Q + (head * 64 + qb * 4 + tt) * 2048 + 1024);
            dma_c4(rw, l16, kvs, ldst);
            dma_c4(rw, l16, kvs + 16384, ldst + 32768);
            waitv<4>(); PIN(q0); PIN(q1); BAR();
            u32x2 frv;
            STAMP(6);
            float m;
            if (pass == 0) {
                f32x16 s = mfma(lds128(lds + l16), q0, zero); s = mfma(lds128(lds + 1024 + l16), q1, s);
                m = s[0];
#pragma unroll
                for (int r = 1; r < 16; ++r) m = fmaxf(m, s[r]);
            } else {
                m = -INFINITY;
#pragma unroll 1
                for (int t = 0; t < 64; ++t) {
                    f32x16 s = mfma(ld_c(rw, l16, WS_KV + (head * 64 + t) * 4096), q0, zero); s = mfma(ld_c(rw, l16 + 1024, WS_KV + (head * 64 + t) * 4096), q1, s);
#pragma unroll
                    for (int r = 0; r < 16; ++r) m = fmaxf(m, s[r]);
                }
            }
            {
                const float n = -max32(m); const unsigned t0 = pk2(n, 0.f) & 0xffffu; const float r1 = n - bflo(t0);
                const unsigned t1 = pk2(r1, 0.f) & 0xffffu; const unsigned t2 = pk2(r1 - bflo(t1), 0.f) & 0xffffu;
                if (h) { q1[2] = t0 | (t1 << 16); q1[3] = t2; }
            }
            o = zero;
            f32x16 sA, sB; u32x4 k0, k1, v0, v1;
            sA = mfma(lds128(rd), q0, zero); sA = mfma(lds128(rd + 1024), q1, sA);
            k0 = lds128(rd + 4096); k1 = lds128(rd + 4096 + 1024);
            v0 = lds128(rd + 2048); v1 = lds128(rd + 3072);
#define EX(P, n) P[n] = __builtin_amdgcn_exp2f(P[n])
#define E8(P, n) do { EX(P, n); EX(P, n + 1); EX(P, n + 2); EX(P, n + 3); EX(P, n + 4); EX(P, n + 5); EX(P, n + 6); EX(P, n + 7); PIN(P); SBAR(); } while (0)
#define PRIO(n) do { __builtin_amdgcn_s_setprio(n); SBAR(); } while (0)
#define TPTR(t) (((((t) >> 2) & 2) ? rd2 : rd) + (((t) >> 2) & 1) * 32768 + ((t) & 3) * 4096)
#define W1T(t) ((t) >= 6 && (t) < 30)
#define STEP(SC, SN, t) do { \
            constexpr bool sync_ = ((t) & 3) == 2 && (t) < 30, ld_ = (t) >= 2 && (t) < 30; \
            constexpr int nw_ = (t) <= 6 ? 4 : 8; \
            constexpr int st_ = (((t) - 2) >> 2) + 3, p_ = ((t) - 2) & 3; \
            if (sync_ && kh) { waitv<nw_>(); BAR(); } \
            PRIO(0); E8(SC, 0); PRIO(1); E8(SC, 8); PRIO(2); \
            if (sync_ && !kh) { waitv<nw_>(); BAR(); } \
            if ((t) == 6) { PIN(frv); if (!flag_ok(frv, ep)) wait_flags(rw, fro, ep); SBAR(); } \
            if ((t) < 31) { SN = mfma(k0, q0, zero); SBAR(); } \
            if ((t) < 2) { dma_co<((t) & 1) * 2048>(rw, l16, kvs + 32768, ldst + 65536); SBAR(); } \
            if ((t) == 2) { frv = ld_flag_async(rw, fro); SBAR(); } \
            if (W1T(t)) { a1[W1T(t) ? (t) - 6 : 0] = ld_c(rw, l16 + p_ * 1024, w1s + (st_ - 4) * 4096); SBAR(); } \
            if ((t) < 31) { SN = mfma(k1, q1, SN); SBAR(); } \
            if ((t) < 2) { dma_co<((t) & 1) * 2048 + 1024>(rw, l16, kvs + 32768, ldst + 65536); SBAR(); } \
            if (ld_) { dma_co<p_ * 1024>(rw, l16, st_ < 8 ? kvs + st_ * 16384 : wos + (st_ - 8) * 32768, ldst + (st_ & 3) * 32768); SBAR(); } \
            o = pv(v0, SC, 0, o); SBAR(); \
            if ((t) < 30) { k0 = lds128(TPTR((t) + 2)); k1 = lds128(TPTR((t) + 2) + 1024); SBAR(); } \
            if ((t) < 31) { v0 = lds128(TPTR((t) + 1) + 2048); SBAR(); } \
            o = pv(v1, SC, 1, o); SBAR(); if ((t) < 31) { v1 = lds128(TPTR((t) + 1) + 3072); SBAR(); } \
            } while (0)
#define STEP4(t) STEP(sA, sB, t); STEP(sB, sA, (t) + 1); STEP(sA, sB, (t) + 2); STEP(sB, sA, (t) + 3)
            CSTAMP(22); STAMP(23);
            STEP4(0); STEP4(4); STEP4(8); STEP4(12); STEP4(16); STEP4(20); STEP4(24); STEP4(28);
            __builtin_amdgcn_s_setprio(0);
            STAMP(7); CSTAMP(24);
            LAS f32x4* red = (LAS f32x4*)(lds + L_RED) + lane;
            LAS float* lsum = (LAS float*)(lds + L_FLAG + 64);
            float amax = fmaxf(fabsf(o[0]), fabsf(o[1]));
#pragma unroll
            for (int r = 2; r < 16; r += 2) amax = fmaxf(amax, fmaxf(fabsf(o[r]), fabsf(o[r + 1])));
            const bool bad = pass == 0 && __any(!(amax <= 1e30f));
            f32x8 mine = kh ? __builtin_shufflevector(o, o, 8, 9, 10, 11, 12, 13, 14, 15) : __builtin_shufflevector(o, o, 0, 1, 2, 3, 4, 5, 6, 7);
            const f32x8 give = kh ? __builtin_shufflevector(o, o, 0, 1, 2, 3, 4, 5, 6, 7) : __builtin_shufflevector(o, o, 8, 9, 10, 11, 12, 13, 14, 15);
            red[w * 128] = __builtin_shufflevector(give, give, 0, 1, 2, 3); red[w * 128 + 64] = __builtin_shufflevector(give, give, 4, 5, 6, 7);
            if (h) lsum[w * 32 + i] = o[12];
            if (lane == 0) ((LAS int*)(lds + L_FLAG))[w] = bad;
            BARRIER();
            const u32x4 f0 = *(const LAS u32x4*)(lds + L_FLAG), f1 = *(const LAS u32x4*)(lds + L_FLAG + 16);
            const f32x4 t0 = red[(w ^ 4) * 128], t1 = red[(w ^ 4) * 128 + 64];
            const float rl = __builtin_amdgcn_rcpf(lsum[tt * 32 + i] + lsum[(tt + 4) * 32 + i]);
            if (__builtin_amdgcn_readfirstlane(f0[0] | f0[1] | f0[2] | f0[3] | f1[0] | f1[1] | f1[2] | f1[3]) == 0) {
                const u32x4 p = {pk2((mine[0] + t0[0]) * rl, (mine[1] + t0[1]) * rl), pk2((mine[2] + t0[2]) * rl, (mine[3] + t0[3]) * rl),
                                 pk2((mine[4] + t1[0]) * rl, (mine[5] + t1[1]) * rl), pk2((mine[6] + t1[2]) * rl, (mine[7] + t1[3]) * rl)};
                st_c(p, rw, l16, WS_O + ((qb * 4 + tt) * 32 + head * 2 + kh) * 1024);
                waitv<0>();
                arrive(lds, 6, 8, rw, WS_F2 + (qb * 2 + (head >> 3)) * 128 + (head & 7) * 8, ep, lane);
                break;
            }
            waitv<0>(); BAR();
        }
    }
    STAMP(8);
    if (jj >= 28) {
        if (b == 255 && w == 0) {
#pragma unroll 1
            for (int n = 0; n < 4; ++n) wait_flags(rw, WS_FR + (n * 64 + lane) * 8, ep);
            if (lane == 0) st_flag(rw, WS_EPOCH, ep);
        }
        STAMP_OUT();
        return;
    }

    float cs1 = 0.f;
    u32x4 a2[28];
    const int w2s = WS_W2 + (2 * c3 * 112 + 14 * w) * 1024;
    {
        const int nt2 = w & 1, rt = (w >> 1) & 1;
        f32x16 acc;
        if (w < 4) {
            const u16* xp = x + (size_t)(r3 * 64 + rt * 32 + i) * D_ + c3 * 64 + 8 * h;
#pragma unroll
            for (int g = 0; g < 4; ++g) {
                const u32x4 xr = *(const u32x4*)(xp + 16 * g);
#pragma unroll
                for (int e = 0; e < 4; ++e) acc[4 * g + e] = nt2 ? bfhi(xr[e]) : bflo(xr[e]);
            }
        }
        auto ld1 = [&]() {
#pragma unroll
            for (int k = 24; k < 28; ++k) a1[k] = ld_c(rw, l16 + (k & 3) * 1024, w1s + (k >> 2) * 4096);
        };
        auto ld2 = [&]() {
#pragma unroll
            for (int k = 0; k < 8; ++k) a2[k] = ld_c(rw, l16, w2s + ((k & 1) * 112 + (k >> 1)) * 1024);
        };
        if (w < 4) { ld1(); ld2(); }
        SBAR();
        float cs2 = 0.f;
#pragma unroll
        for (int k = 0; k < 24; ++k) { cs1 = dot2(a1[k][0], ONES, cs1); cs2 = dot2(a1[k][1], ONES, cs2); cs1 = dot2(a1[k][2], ONES, cs1); cs2 = dot2(a1[k][3], ONES, cs2); }
        PIN(cs1); PIN(cs2); PIN(acc);
        if (w == 7) wait_flags(rw, WS_F2 + ((r3 >> 1) * 2 + ((lane >> 3) & 1)) * 128 + (lane & 7) * 8, ep);
        BAR();
        STAMP(9);
        dma_c4(rw, l16, WS_O + r3 * 65536 + w * 8192, lbase + 65536 + w * 8192);
        dma_c4(rw, l16, WS_O + r3 * 65536 + w * 8192 + 4096, lbase + 65536 + w * 8192 + 4096);
        waitv<0>(); BAR();
        STAMP(10);
        if (w < 4) {
            const LAS char* pa = lds + nt2 * 32768 + l16; const LAS char* pb = lds + 65536 + rt * 32768 + l16;
            u32x4 fa[4], fb[4];
#pragma unroll
            for (int k = 0; k < 4; ++k) { fa[k] = lds128(pa + k * 1024); fb[k] = lds128(pb + k * 1024); }
            SBAR();
#pragma unroll
            for (int k = 0; k < 32; ++k) {
                acc = mfma(fa[k & 3], fb[k & 3], acc); SBAR();
                if (k < 28) { fa[k & 3] = lds128(pa + (k + 4) * 1024); fb[k & 3] = lds128(pb + (k + 4) * 1024); SBAR(); }
            }
            const int rtile = r3 * 2 + rt, nt = 2 * c3 + nt2;
            const u32x4 p0 = pack8(acc, 0), p1 = pack8(acc, 1);
            st_c(p0, rw, l16, WS_H + (rtile * 28 + 2 * nt) * 1024); st_c(p1, rw, l16 + 1024, WS_H + (rtile * 28 + 2 * nt) * 1024);
            float s = 0.f, q = 0.f, s2 = 0.f, q2 = 0.f;
#pragma unroll
            for (int e = 0; e < 4; ++e) { s = dot2(p0[e], ONES, s); q = dot2(p0[e], p0[e], q); s2 = dot2(p1[e], ONES, s2); q2 = dot2(p1[e], p1[e], q2); }
            s = sum32(s + s2); q = sum32(q + q2);
            __builtin_amdgcn_raw_buffer_store_b32(__float_as_uint(h ? q : s), rw, lane * 4, WS_ST + (rtile * 14 + nt) * 256, 16);
            *(LAS u32x4*)(lds + L_HS + w * 2048 + l16) = p0; *(LAS u32x4*)(lds + L_HS + w * 2048 + 1024 + l16) = p1;
            STAMP(11);
            waitv<0>();
            arrive(lds, 0, 4, rw, WS_F3 + r3 * 128 + c3 * 8, ep, lane);
        } else { ld1(); ld2(); }
#pragma unroll
        for (int k = 24; k < 28; ++k) { cs1 = dot2(a1[k][0], ONES, cs1); cs2 = dot2(a1[k][1], ONES, cs2); cs1 = dot2(a1[k][2], ONES, cs1); cs2 = dot2(a1[k][3], ONES, cs2); }
        cs1 = sum32(cs1 + cs2);
    }
    {
        const int ft = 8 * c3 + w;
        const unsigned bias = b1[(ft >> 1) * 64 + 2 * i + (ft & 1)];
        unsigned ch, cl; split2(cs1, ch, cl);
        u32x4 ae = {h ? 0u : (ch | (cl << 16)), h ? 0u : (ch | (cl << 16)), h ? 0u : (bias | (bias << 16)), 0u};
        PIN(ae);
        if (w == 7) wait_flags(rw, WS_F3 + r3 * 128 + (lane < 7 ? lane : 0) * 8, ep);
        BAR();
        STAMP(12);
        dma_c4(rw, l16, WS_H + r3 * 57344 + w * 7168, lbase + w * 7168);
#pragma unroll
        for (int p = 4; p < 7; ++p) dma_c(rw, l16, WS_H + r3 * 57344 + w * 7168 + p * 1024, lbase + w * 7168 + p * 1024);
        if (w < 7) dma_c(rw, l16, WS_ST + r3 * 7168 + w * 1024, lbase + 57344 + w * 1024);
        waitv<0>(); BARRIER();
        STAMP(13);
        u32x4 fb[4];
#pragma unroll
        for (int k = 0; k < 4; ++k) fb[k] = lds128(lds + k * 1024 + l16);
#pragma unroll
        for (int rt = 0; rt < 2; ++rt) {
            f32x16 acc = zero;
            const LAS float* sp = (const LAS float*)(lds + 57344) + rt * 14 * 64 + lane;
            float st = 0.f, sv[14];
            u32x4 be; float g1, g2, sd;
            if (rt) __builtin_amdgcn_s_setprio(0); else __builtin_amdgcn_s_setprio(1);
            SBAR();
#pragma unroll
            for (int k = 0; k < 28; ++k) {
                acc = mfma(a1[k], fb[k & 3], acc); SBAR();
                if (rt * 28 + k + 4 < 56) fb[k & 3] = lds128(lds + (rt * 28 + k + 4) * 1024 + l16);
                if (k < 7) { sv[2 * k] = sp[2 * k * 64]; sv[2 * k + 1] = sp[(2 * k + 1) * 64]; }
                if (k >= 4 && k < 11) { st += sv[2 * (k - 4)] + sv[2 * (k - 4) + 1]; PIN(st); }
                if (k == 12) {
                    float sum, sq; both32(st, sum, sq);
                    const float mean = sum * (1.f / D_), var = fmaxf(sq * (1.f / D_) - mean * mean, 0.f);
                    const float rs1 = __builtin_amdgcn_rsqf(var + EPS);
                    const float rs = rs1 * __builtin_amdgcn_rsqf(var * rs1 * rs1 + EPS);
                    sd = __builtin_amdgcn_rcpf(rs); g1 = -2.3022081981f * rs; g2 = -0.1029432397f * rs * rs * rs;
                    PIN(g1); PIN(g2);
                    unsigned mh, ml, sh, sl; split2(-mean, mh, ml); split2(sd, sh, sl);
                    be = (u32x4){h ? 0u : (mh | (mh << 16)), h ? 0u : (ml | (ml << 16)), h ? 0u : (sh | (sl << 16)), 0u};
                    acc = mfma(ae, be, acc);
                }
                if (rt == 0 && k >= 8 && !(k & 1)) { const int q = 8 + (k - 8) / 2; a2[q] = ld_c(rw, l16, w2s + ((q & 1) * 112 + (q >> 1)) * 1024); }
                if (rt == 1 && k >= 3 && k < 23 && (k & 1)) { const int q = 18 + (k - 3) / 2; a2[q] = ld_c(rw, l16, w2s + ((q & 1) * 112 + (q >> 1)) * 1024); }
                if (rt == 1 && k == 23) { waitv<10>(); arrive(lds, 1, 8, rw, WS_F4 + (r3 * 2) * 128 + c3 * 8, ep, lane); }
                SBAR();
            }
#pragma unroll
            for (int r = 0; r < 16; ++r) {
                const float a = acc[r], e = __builtin_amdgcn_exp2f(a * __builtin_fmaf(a * a, g2, g1));
                acc[r] = a * __builtin_amdgcn_rcpf(__builtin_fmaf(e, sd, sd));
            }
            const int dst = WS_HID + ((r3 * 2 + rt) * 112 + 2 * ft) * 1024;
            st_c(pack8(acc, 0), rw, l16, dst); st_c(pack8(acc, 1), rw, l16 + 1024, dst);
            SBAR();
            if (rt == 0) STAMP(14);
        }
        STAMP(15);
        waitv<0>();
        arrive(lds, 2, 8, rw, WS_F4 + (r3 * 2 + 1) * 128 + c3 * 8, ep, lane);
    }
    {
        const int col = c3 * 64 + 2 * ((2 * w & 3) + 8 * (w >> 1) + 4 * h);
        const u32x2 bb = *(const u32x2*)(b2 + col);
        const int f4o = WS_F4 + r3 * 256 + (lane < 7 ? lane : 0) * 8;
        u32x4 bf[14];
        volatile LAS unsigned* seen = (volatile LAS unsigned*)(lds + L_CNT) + 5;
        if (w == 0) { wait_flags(rw, f4o, ep); *seen = 1u; }
        else {
#pragma unroll 1
            for (int spin = 0; spin < (1 << 24) && *seen == 0u; ++spin) __builtin_amdgcn_s_sleep(1);
        }
        asm volatile("" ::: "memory");
        STAMP(16);
        u32x2 fB;
#pragma unroll
        for (int k = 0; k < 14; ++k) { bf[k] = ld_c(rw, l16, WS_HID + (r3 * 224 + 14 * w + k) * 1024); if (k == FBPOS) fB = ld_flag(rw, f4o + 128); SBAR(); }
        if (!flag_ok(fB, ep)) wait_flags(rw, f4o + 128, ep);
        STAMP(17);
#pragma unroll
        for (int rt = 0; rt < 2; ++rt) {
            f32x16 acc0 = zero, acc1 = zero;
#pragma unroll
            for (int k = 0; k < 14; ++k) {
                acc0 = mfma(a2[2 * k], bf[k], acc0); acc1 = mfma(a2[2 * k + 1], bf[k], acc1); SBAR();
                if (rt == 0) { bf[k] = ld_c(rw, l16, WS_HID + (r3 * 224 + 112 + 14 * w + k) * 1024); SBAR(); }
            }
            if (rt == 0) STAMP(18); else STAMP(20);
            LAS f32x4* red = (LAS f32x4*)(lds + (rt ? 0 : 65536)) + lane;
#pragma unroll
            for (int p = 0; p < 8; ++p) red[(p * 8 + w) * 64] = (f32x4){acc0[2 * p], acc1[2 * p], acc0[2 * p + 1], acc1[2 * p + 1]};
            SBAR();
            if (rt == 0) STAMP(19);
        }
        BARRIER();
#pragma unroll
        for (int rt = 0; rt < 2; ++rt) {
            const LAS f32x4* red = (const LAS f32x4*)(lds + (rt ? 0 : 65536)) + lane;
            f32x4 r = red[(w * 8) * 64];
#pragma unroll
            for (int q = 1; q < 8; ++q) r += red[(w * 8 + q) * 64];
            const LAS char* hp = lds + L_HS + rt * 4096 + (w >> 2) * 1024 + l16 + (w & 3) * 4;
            const unsigned h0 = *(const LAS unsigned*)hp, h1 = *(const LAS unsigned*)(hp + 2048);
            const u32x2 res = {pk2(r[0] + bflo(bb[0]) + bflo(h0), r[1] + bfhi(bb[0]) + bflo(h1)), pk2(r[2] + bflo(bb[1]) + bfhi(h0), r[3] + bfhi(bb[1]) + bfhi(h1))};
            *(u32x2*)(out + (size_t)(r3 * 64 + rt * 32 + i) * D_ + col) = res;
        }
    }
    STAMP(21);
    STAMP_OUT();
}

extern "C" void kernel_launch(void* const* d_in, const int* in_sizes, int n_in, void* d_out, int out_size, void* d_ws, size_t ws_size, hipStream_t stream) {
    (void)hipFuncSetAttribute((const void*)fused_block, hipFuncAttributeMaxDynamicSharedMemorySize, LDS_BYTES);
    fused_block<<<256, 512, LDS_BYTES, stream>>>((const u16*)d_in[0], (const u16*)d_in[1], (const u16*)d_in[2], (const u16*)d_in[4], (const u16*)d_in[5],
                                                 (const u16*)d_in[6], (const u16*)d_in[7], (u16*)d_out, (char*)d_ws);
}
```

```cpp
#include <hip/hip_runtime.h>
#include <stdint.h>

typedef unsigned short u16;
typedef unsigned long long u64;
typedef __bf16 bf16x2 __attribute__((ext_vector_type(2)));
typedef __bf16 bf16x8 __attribute__((ext_vector_type(8)));
typedef __bf16 bf16x16 __attribute__((ext_vector_type(16)));
typedef float f32x2 __attribute__((ext_vector_type(2)));
typedef float f32x4 __attribute__((ext_vector_type(4)));
typedef float f32x8 __attribute__((ext_vector_type(8)));
typedef float f32x16 __attribute__((ext_vector_type(16)));
typedef unsigned u32x2 __attribute__((ext_vector_type(2)));
typedef unsigned u32x4 __attribute__((ext_vector_type(4)));
typedef short s16x4 __attribute__((ext_vector_type(4)));
typedef __amdgpu_buffer_rsrc_t rsrc_t;
#define LAS __attribute__((address_space(3)))
#define DI __device__ __forceinline__
#define SBAR() __builtin_amdgcn_sched_barrier(0)
#define PIN(x) asm volatile("" : "+v"(x))

#define USEBAR 0
#define NW1 24
#define NE2 4
#define B1SLEEP 4
#define WARM2 0
#define ROTK 1
#define E3SLEEP 8
#define W1MODE 1
constexpr int D_ = 448, F_ = 1792;
constexpr float EPS = 1e-5f;
constexpr int WS_EPOCH = 0;
constexpr int WS_F1 = 1024;
constexpr int WS_FR = WS_F1 + 96 * 128;
constexpr int WS_FP = WS_FR + 2048;
constexpr int WS_ST = 32768;
constexpr int WS_Q = WS_ST + 64 * 14 * 256;
constexpr int WS_KV = WS_Q + 16 * 64 * 2048;
constexpr int WS_O = WS_KV + 16 * 64 * 4096;
constexpr int WS_H = WS_O + 64 * 32 * 1024;
constexpr int WS_W1 = WS_H + 64 * 28 * 1024;
constexpr int WS_W2 = WS_W1 + 56 * 28 * 1024;
constexpr int WS_WO = WS_W2 + 14 * 112 * 1024;
constexpr int WS_HID = WS_WO + 14 * 32 * 1024;
constexpr int WS_END = WS_HID + 64 * 112 * 1024;
static_assert(WS_FP + 2048 <= WS_ST, "flags overlap");
constexpr int OOB = 0x40000000;

constexpr int XCH = 16384, WCH = 12288, CHB = XCH + WCH;
constexpr int L_MISC = 5 * CHB;
constexpr int L_CS = L_MISC, L_XE = L_MISC + 3072, L_CW = L_XE + 4096, L_RT = L_CW + 768;
constexpr int L_VT = 2 * CHB;
constexpr int L_RED = 131072;
constexpr int L_FLAG = L_RED + 16384;
constexpr int L_HS = L_RED + 16384 + 64 + 2048;
constexpr int L_B2 = L_HS + 8192;
constexpr int L_CNT = 163840 - 1024 - 64;
constexpr int L_DUMP = 163840 - 1024;
constexpr int LDS_BYTES = 163840;

#define STAMP_DECL
#define STAMP(i)
#define CSTAMP(i)
#define TRY(i)
#define STAMP_OUT()

DI f32x16 mfma(u32x4 a, u32x4 b, f32x16 c) {
    return __builtin_amdgcn_mfma_f32_32x32x16_bf16(__builtin_bit_cast(bf16x8, a), __builtin_bit_cast(bf16x8, b), c, 0, 0, 0);
}
DI f32x4 mfma16(u32x4 a, u32x4 b, f32x4 c) {
    return __builtin_amdgcn_mfma_f32_16x16x32_bf16(__builtin_bit_cast(bf16x8, a), __builtin_bit_cast(bf16x8, b), c, 0, 0, 0);
}
DI f32x4 mfma4(unsigned a0, unsigned a1, unsigned b0, unsigned b1, f32x4 c) {
    return __builtin_amdgcn_mfma_f32_4x4x4bf16_1k(__builtin_bit_cast(s16x4, (u32x2){a0, a1}), __builtin_bit_cast(s16x4, (u32x2){b0, b1}), c, 0, 0, 0);
}
DI f32x16 pv(u32x4 v, const f32x16& p, int s, f32x16 o) {
    f32x8 ph = {p[8 * s], p[8 * s + 1], p[8 * s + 2], p[8 * s + 3], p[8 * s + 4], p[8 * s + 5], p[8 * s + 6], p[8 * s + 7]};
    return __builtin_amdgcn_smfmac_f32_32x32x32_bf16(__builtin_bit_cast(bf16x8, v), __builtin_bit_cast(bf16x16, ph), o, 0xDDDD, 0, 0);
}
DI unsigned pk2(float lo, float hi) { f32x2 v = {lo, hi}; return __builtin_bit_cast(unsigned, __builtin_convertvector(v, bf16x2)); }
DI float bflo(unsigned u) { return __uint_as_float(u << 16); }
DI float bfhi(unsigned u) { return __uint_as_float(u & 0xffff0000u); }
DI float dot2(unsigned a, unsigned b, float c) { return __builtin_amdgcn_fdot2_f32_bf16(__builtin_bit_cast(bf16x2, a), __builtin_bit_cast(bf16x2, b), c, false); }
DI void both32(float v, float& lo, float& hi) {
    auto r = __builtin_amdgcn_permlane32_swap(__float_as_uint(v), __float_as_uint(v), false, false);
    lo = __uint_as_float(r[0]); hi = __uint_as_float(r[1]);
}
DI float sum32(float v) { float a, b; both32(v, a, b); return a + b; }
DI float max32(float v) { float a, b; both32(v, a, b); return fmaxf(a, b); }
DI u32x4 lds128(const LAS char* p) { return *(const LAS u32x4*)p; }
DI u32x2 ldstr(const LAS char* p) { return __builtin_bit_cast(u32x2, __builtin_amdgcn_ds_read_tr16_b64_v4i16((LAS s16x4*)p)); }
DI void split2(float v, unsigned& hi, unsigned& lo) { hi = pk2(v, 0.f) & 0xffffu; lo = pk2(v - bflo(hi), 0.f) & 0xffffu; }
DI u32x4 pack8(const f32x16& a, int s) {
    u32x4 r;
    r[0] = pk2(a[8 * s + 0], a[8 * s + 1]); r[1] = pk2(a[8 * s + 2], a[8 * s + 3]);
    r[2] = pk2(a[8 * s + 4], a[8 * s + 5]); r[3] = pk2(a[8 * s + 6], a[8 * s + 7]);
    return r;
}
DI void dma(rsrc_t rs, int voff, int soff, unsigned lds_addr) {
    asm volatile("s_mov_b32 m0, %0\n\ts_nop 3\n\tbuffer_load_dwordx4 %1, %2, %3 offen lds" :: "s"(lds_addr), "v"(voff), "s"(rs), "s"(soff) : "memory");
}
DI void dma_c(rsrc_t rs, int voff, int soff, unsigned lds_addr) {
    asm volatile("s_mov_b32 m0, %0\n\ts_nop 3\n\tbuffer_load_dwordx4 %1, %2, %3 offen sc1 lds" :: "s"(lds_addr), "v"(voff), "s"(rs), "s"(soff) : "memory");
}
DI void dma_c4(rsrc_t rs, int voff, int soff, unsigned lds_addr) {
    asm volatile("s_mov_b32 m0, %0\n\ts_nop 3\n\tbuffer_load_dwordx4 %1, %2, %3 offen sc1 lds\n\tbuffer_load_dwordx4 %1, %2, %3 offen offset:1024 sc1 lds\n\t"
                 "buffer_load_dwordx4 %1, %2, %3 offen offset:2048 sc1 lds\n\tbuffer_load_dwordx4 %1, %2, %3 offen offset:3072 sc1 lds"
                 :: "s"(lds_addr), "v"(voff), "s"(rs), "s"(soff) : "memory");
}
DI u32x4 ld_c(rsrc_t rs, int voff, int soff) { return __builtin_amdgcn_raw_buffer_load_b128(rs, voff, soff, 16); }
DI void st_c(u32x4 v, rsrc_t rs, int voff, int soff) { __builtin_amdgcn_raw_buffer_store_b128(v, rs, voff, soff, 16); }
DI u32x2 ld_flag(rsrc_t rs, int voff) { asm volatile("" ::: "memory"); return __builtin_amdgcn_raw_buffer_load_b64(rs, voff, 0, 16); }
DI void st_flag(rsrc_t rs, int voff, u32x2 e) { __builtin_amdgcn_raw_buffer_store_b64(e, rs, voff, 0, 16); }
template <int N> DI void waitv() {
    asm volatile("s_waitcnt vmcnt(%0)" :: "n"(N) : "memory");
    if (N == 0) __builtin_amdgcn_s_waitcnt(0x0F70);
}
#define BAR() asm volatile("s_barrier" ::: "memory")
#define BARRIER() asm volatile("s_waitcnt lgkmcnt(0)\n\ts_barrier" ::: "memory")
DI bool flag_ok(u32x2 f, u32x2 e) { return __all(f[0] == e[0] && f[1] == e[1]); }
DI void wait_flags(rsrc_t rs, int voff, u32x2 e) {
#pragma unroll 1
    for (int spin = 0; spin < (1 << 18); ++spin) {
        if (flag_ok(ld_flag(rs, voff), e)) break;
        __builtin_amdgcn_s_sleep(1);
    }
    asm volatile("" ::: "memory");
}
DI void arrive(unsigned cnt_addr, unsigned inc, unsigned n, rsrc_t rs, int foff, u32x2 e, int lane, int nl = 1) {
    unsigned old;
    asm volatile("s_mov_b64 exec, 1\n\tds_add_rtn_u32 %0, %1, %2\n\ts_mov_b64 exec, -1\n\ts_waitcnt lgkmcnt(0)" : "=&v"(old) : "v"(cnt_addr), "v"(inc) : "memory");
    if (__builtin_amdgcn_readfirstlane(old) + inc == n && lane < nl) st_flag(rs, foff, e);
}
template <int OFF> DI void dma_co(rsrc_t rs, int voff, int soff, unsigned lds_addr) {
    asm volatile("s_mov_b32 m0, %0\n\ts_nop 3\n\tbuffer_load_dwordx4 %1, %2, %3 offen offset:%4 sc1 lds" :: "s"(lds_addr), "v"(voff), "s"(rs), "s"(soff), "n"(OFF) : "memory");
}
DI u32x4 ld_c_async(rsrc_t rs, int voff, int soff) { u32x4 r; asm volatile("s_nop 4\n\tbuffer_load_dwordx4 %0, %1, %2, %3 offen sc1" : "=v"(r) : "v"(voff), "s"(rs), "s"(soff) : "memory"); return r; }
DI u32x2 ld_flag_async(rsrc_t rs, int voff) { u32x2 r; asm volatile("buffer_load_dwordx2 %0, %1, %2, 0 offen sc1" : "=v"(r) : "v"(voff), "s"(rs) : "memory"); return r; }
DI unsigned pmax(unsigned m, u32x4 v) { return max(max(max(max(v[0], v[1]), m), v[2]), v[3]); }
DI bool poisoned(unsigned m) { return __any(m == 0xffffffffu); }
DI void spin_lds(volatile LAS unsigned* p, unsigned tgt) {
#pragma unroll 1
    for (int spin = 0; spin < (1 << 24); ++spin) { if (__builtin_amdgcn_readfirstlane(*p) >= tgt) break; __builtin_amdgcn_s_sleep(1); }
}
DI float gelu_tanh(float x) {
    const float t = __builtin_fmaf(x * x, -0.1029432397f, -2.3022081981f);
    const float e = __builtin_amdgcn_exp2f(x * t);
    return x * __builtin_amdgcn_rcpf(1.f + e);
}

__global__ __launch_bounds__(512, 2) void fused_block(const u16* __restrict__ x, const u16* __restrict__ wqkv, const u16* __restrict__ wo,
                                                       const u16* __restrict__ w1, const u16* __restrict__ b1, const u16* __restrict__ w2,
                                                       const u16* __restrict__ b2, u16* __restrict__ out, char* __restrict__ ws) {
    extern __shared__ __attribute__((aligned(16))) char smem[];
    LAS char* lds = (LAS char*)smem;
    const unsigned lbase = (unsigned)(uintptr_t)lds;
    const int lane = threadIdx.x & 63, w = __builtin_amdgcn_readfirstlane(threadIdx.x >> 6);
    const int i = lane & 31, h = lane >> 5, l16 = lane * 16;
    const int b = blockIdx.x, xcd = b & 7, jj = b >> 3;
    STAMP_DECL; STAMP(0);
    const int rb = 4 * (xcd >> 1) + (jj & 3), ct = 8 * (xcd & 1) + (jj >> 2);
    const int tt = w & 3;
        const rsrc_t rx = __builtin_amdgcn_make_buffer_rsrc((void*)x, 0, 2048 * 896, 0x00020000);
        const rsrc_t rq = __builtin_amdgcn_make_buffer_rsrc((void*)wqkv, 0, 448 * 2688, 0x00020000);
        const int vx0 = (lane >> 3) * 896 + (((lane & 7) ^ (lane >> 4)) << 4), vx1 = vx0 ^ 64;
        int vw[3]; u64 mk[3];
#pragma unroll
        for (int p = 0; p < 3; ++p) { const int q = (3 * tt + p) * 64 + lane, row = (q * 43691) >> 19; vw[p] = row * (2688 - 192) + q * 16; mk[p] = __ballot(q - row * 12 != 11); }
#define ROT ((ct + 2 * rb) % 7)
        int cg = ROT;
        const int sx0 = (rb * 128 + 32 * tt) * 896, sw0 = (168 * ct) & ~15;
        unsigned ldn = lbase;
        auto issue1 = [&](int p) {
            if (p < 4) dma(rx, (p & 1) ? vx1 : vx0, sx0 + p * 7168 + cg * 128, ldn + (4 * tt + p) * 1024);
            else
                asm volatile("s_mov_b32 m0, %0\n\ts_mov_b64 exec, %4\n\ts_nop 2\n\tbuffer_load_dwordx4 %1, %2, %3 offen lds\n\ts_mov_b64 exec, -1"
                             :: "s"(ldn + XCH + (3 * tt + p - 4) * 1024), "v"(vw[p - 4]), "s"(rq), "s"(sw0 + cg * (64 * 2688)), "s"(mk[p - 4]) : "memory");
        };
    if (w >= 4) {
#pragma unroll 1
        for (int c = 0; c < 2; ++c) {
#pragma unroll
            for (int p = 0; p < 7; ++p) issue1(p);
            cg = cg == 6 ? 0 : cg + 1; ldn += CHB;
        }
    }
    ;
    SBAR();
    const rsrc_t rw = __builtin_amdgcn_make_buffer_rsrc(ws, 0, WS_END, 0x00020000);
    const f32x16 zero = {};
    const unsigned ONES = 0x3f803f80u;
    u32x2 ep;
    u32x4 rte[2], rto[2]; int rdst[2], rts[2];
    if (w >= 4) {
        __builtin_amdgcn_s_setprio(3);
        const int r16 = lane & 15, g16 = lane >> 4;
        const LAS char* xp = lds + (32 * tt + r16) * 128;
        const LAS char* wp = lds + XCH + (32 * (tt >> 1) + 8 * g16 + (r16 >> 2)) * 192 + ((168 * ct) & 15) + ((tt & 1) * 16 + (lane & 3) * 4) * 2;
        const u32x4 ones4 = {ONES, ONES, ONES, ONES};
        f32x4 sq[2] = {{}, {}}, cs[3] = {{}, {}, {}};
        u32x4 xf[4], wf[3];
        int sl = 0;
        auto rd = [&](int p) {
            if (p < 4) xf[p] = lds128(xp + sl + (p & 1) * 2048 + ((((p >> 1) * 4 + g16) ^ (r16 >> 1)) << 4));
            else { const u32x2 t0 = ldstr(wp + sl + (p - 4) * 56), t1 = ldstr(wp + sl + (p - 4) * 56 + 768); wf[p - 4] = (u32x4){t0[0], t0[1], t1[0], t1[1]}; }
            SBAR();
        };
        auto acc_ = [&](int p) {
            if (p < 4) sq[p & 1] = mfma16(xf[p], xf[p], sq[p & 1]);
            else cs[p - 4] = mfma16(ones4, wf[p - 4], cs[p - 4]);
            SBAR();
        };
        waitv<7>(); BAR();
        STAMP(1);
#pragma unroll
        for (int p = 0; p < 7; ++p) { issue1(p); rd(p); }
        cg = cg == 6 ? 0 : cg + 1; ldn += CHB;
#pragma unroll
        for (int p = 0; p < 7; ++p) issue1(p);
        cg = cg == 6 ? 0 : cg + 1; ldn += CHB;
#pragma unroll 1
        for (int c = 4; c < 7; ++c) {
            waitv<14>(); BAR();
            sl += CHB;
#pragma unroll
            for (int p = 0; p < 7; ++p) { issue1(p); acc_(p); rd(p); }
            cg = cg == 6 ? 0 : cg + 1; ldn = ldn == lbase + 4 * CHB ? lbase : ldn + CHB;
        }
        unsigned rv[2][8];
        ;
#pragma unroll
        for (int n = 0; n < 3; ++n) {
            rsrc_t rr; int rst, rs0, v0, v1;
            if (n < 2) {
                const u32x4 pa = lds128(lds + L_RT + (tt * 2 + n) * 32), pb = lds128(lds + L_RT + (tt * 2 + n) * 32 + 16);
                const u64 src = (u64)(unsigned)__builtin_amdgcn_readfirstlane(pa[0]) | ((u64)(unsigned)__builtin_amdgcn_readfirstlane(pa[1]) << 32);
                rr = __builtin_amdgcn_make_buffer_rsrc((void*)src, 0, __builtin_amdgcn_readfirstlane(pa[2]), 0x00020000);
                rst = __builtin_amdgcn_readfirstlane(pa[3]); rs0 = __builtin_amdgcn_readfirstlane(pb[0]);
                rdst[n] = __builtin_amdgcn_readfirstlane(pb[1]); rts[n] = __builtin_amdgcn_readfirstlane(pb[2]);
                v0 = 4 * h * rst + 4 * i; v1 = v0 | (h ? pb[3] : 0);
            }
            if (n == 0) waitv<14>(); else if (n == 1) waitv<15>(); else waitv<16>();
            if (n == 2) ;
            BAR();
            if (n == 2) ;
            sl = sl == 4 * CHB ? 0 : sl + CHB;
#pragma unroll
            for (int p = 0; p < 8; ++p) {
                if (p < 7) { acc_(p); rd(p); }
                if (n < 2) { rv[n][p] = __builtin_amdgcn_raw_buffer_load_b32(rr, p < 4 ? v0 : v1, rs0 + rst * (8 * (p >> 2) + (p & 3)), 0); SBAR(); }
            }
        }
#pragma unroll
        for (int p = 0; p < 7; ++p) acc_(p);
        LAS float* csb = (LAS float*)(lds + L_CS);
        if (lane < 16) {
#pragma unroll
            for (int s = 0; s < 3; ++s) csb[(s * 2 + (tt >> 1)) * 32 + (tt & 1) * 16 + lane] = cs[s][0];
        }
#pragma unroll
        for (int tb = 0; tb < 2; ++tb) {
            const float q01 = (lane & 1) ? sq[tb][1] : sq[tb][0], q23 = (lane & 1) ? sq[tb][3] : sq[tb][2];
            if (g16 == (r16 >> 2)) ((LAS float*)(lds + L_XE))[tt * 32 + tb * 16 + r16] = (lane & 2) ? q23 : q01;
        }
        STAMP(2);
        BARRIER();
        __builtin_amdgcn_s_setprio(0);
        ep = *(const LAS u32x2*)(lds + L_CNT + 32);
#pragma unroll
        for (int n = 0; n < 2; ++n) {
#pragma unroll
            for (int j = 0; j < 4; ++j) { rte[n][j] = __builtin_amdgcn_perm(rv[n][2 * j + 1], rv[n][2 * j], 0x05040100u); rto[n][j] = __builtin_amdgcn_perm(rv[n][2 * j + 1], rv[n][2 * j], 0x07060302u); }
            if (n == 0 || tt < 3) { st_c(rte[n], rw, l16, rdst[n]); st_c(rto[n], rw, l16, rdst[n] + rts[n]); }
        }
        STAMP(3);
        waitv<0>();
        STAMP(4);
        arrive(lbase + L_CNT + 12, 1, 4, rw, WS_FR + b * 8, ep, lane);
    } else {
        if (w == 0) {
            u64 pc; int len;
            asm volatile("s_getpc_b64 %0\n87:\n\ts_mov_b32 %1, 88f-87b" : "=s"(pc), "=s"(len));
            const rsrc_t rc = __builtin_amdgcn_make_buffer_rsrc((void*)(pc & ~127ull), 0, len, 0x00020000);
            dma(rc, l16, jj * 1024, lbase + L_DUMP);
            if (lane < 16 && (lane >> 1) != 4) ((LAS unsigned*)(lds + L_CNT))[lane] = 0u;
        }
        if (w == 3) {
            const u64 e = __builtin_bit_cast(u64, __builtin_amdgcn_raw_buffer_load_b64(rw, 0, WS_EPOCH, 16)) + 1;
            if (lane == 0) *(LAS u64*)(lds + L_CNT + 32) = e;
        }
        if (lane < 2) {
            const int u = 7 * b + (lane && tt < 3 ? tt + 4 : tt);
            const bool is1 = u < 784, is2 = u < 1568;
            const int q = is1 ? u : (is2 ? u - 784 : u - 1568), nks = is1 ? 28 : (is2 ? 112 : 32);
            const int win = is1 ? q / 28 : (is2 ? q / 112 : q >> 5), ks = q - win * nks;
            const int r0 = is2 ? 64 * (ks >> 2) + ((ks >> 1) & 1) + 32 * (ks & 1) : 28 * (ks >> 1) + 16 * (ks & 1);
            const u64 src = (u64)(is1 ? w1 : (is2 ? w2 : wo));
            LAS u32x4* pr = (LAS u32x4*)(lds + L_RT + (tt * 2 + lane) * 32);
            pr[0] = (u32x4){(unsigned)src, (unsigned)(src >> 32), (unsigned)(is2 ? 2 * D_ * F_ : 2 * D_ * D_), (unsigned)(is1 ? 4 * F_ : (is2 ? 4 * D_ : 2 * D_))};
            pr[1] = (u32x4){(unsigned)(r0 * (is1 ? 2 * F_ : 2 * D_) + win * 128), (unsigned)((is1 ? WS_W1 : (is2 ? WS_W2 : WS_WO)) + (2 * win * nks + (!is1 && is2 ? ((ks >> 3) & 1) * 56 + (ks >> 4) * 8 + (ks & 7) : ks)) * 1024),
                            (unsigned)(nks * 1024), (!is2 && (ks & 1)) ? 0x40000000u : 0u};
        }
        const LAS char* xp[4];
#pragma unroll
        for (int ks = 0; ks < 4; ++ks) xp[ks] = lds + (32 * tt + i) * 128 + (((2 * ks + h) ^ ((i >> 1) & 7)) << 4);
        const int wcol = ((168 * ct) & 15) + (((lane >> 4) & 1) * 16 + (lane & 3) * 4) * 2;
        const LAS char* wp = lds + XCH + (8 * h + ((lane & 15) >> 2)) * 192 + wcol;
        const LAS char* wp2 = wp + ((lane & 19) == 19 ? 176 - wcol : 112);
        *(LAS u32x2*)(lds + tt * CHB + XCH + lane * 192 + 176) = (u32x2){ONES, ONES};
        if (w == 0) *(LAS u32x2*)(lds + 4 * CHB + XCH + lane * 192 + 176) = (u32x2){ONES, ONES};
        unsigned scl[3], pd2[3], pd3[3]; int dst[3];
#pragma unroll
        for (int s = 0; s < 3; ++s) {
            const int sec = 3 * ct + s, tl = (sec & 15) * 64 + rb * 4 + tt;
            scl[s] = sec < 16 ? __builtin_bit_cast(unsigned, 0.18898223650461361f * 1.4426950408889634f) : 0x3f800000u;
            pd2[s] = sec < 16 ? 0u : (sec < 32 ? ONES : 0x3f80u); pd3[s] = (sec >= 16 && sec < 32) ? 0x3f80u : 0u;
            dst[s] = sec < 16 ? WS_Q + tl * 2048 : WS_KV + tl * 4096 + (sec >= 32 ? 2048 : 0);
            asm volatile("" : "+s"(scl[s]), "+s"(pd2[s]), "+s"(pd3[s]), "+s"(dst[s]));
        }
        LAS char* vtw = lds + L_VT + w * 6912 + i * 72 + h * 8;
        const LAS char* vtr = lds + L_VT + w * 6912 + (8 * h + ((lane & 15) >> 2)) * 72 + ((lane >> 4) & 1) * 32 + (lane & 3) * 8;
        int f1o = WS_F1 + ((3 * ct + lane) * 2 + (rb >> 3)) * 128 + (rb & 7) * 8;
        PIN(vtw); PIN(vtr); PIN(f1o);
        f32x16 acc[3] = {zero, zero, zero};
        PIN(acc[0]); PIN(acc[1]); PIN(acc[2]);
        u32x4 xf[4], wf[4][3];
        auto rdw = [&](int ks, int s, const LAS char* p) {
            const u32x2 t0 = ldstr(p + ks * 3072 + (s & 1) * 56), t1 = ldstr(p + ks * 3072 + (s & 1) * 56 + 768);
            wf[ks][s] = (u32x4){t0[0], t0[1], t1[0], t1[1]};
        };
        BARRIER();
        STAMP(1);
#pragma unroll
        for (int ks = 0; ks < 4; ++ks) { xf[ks] = lds128(xp[ks]); rdw(ks, 0, wp); rdw(ks, 1, wp); rdw(ks, 2, wp2); SBAR(); }
        int sl = CHB;
#pragma unroll 1
        for (int c = 1; c < 7; ++c) {
            BAR();
            const LAS char* p = wp + sl; const LAS char* p2 = wp2 + sl;
#pragma unroll
            for (int ks = 0; ks < 4; ++ks)
#pragma unroll
                for (int s = 0; s < 3; ++s) {
                    acc[s] = mfma(wf[ks][s], xf[ks], acc[s]); SBAR();
                    if (s == 2) xf[ks] = lds128(xp[ks] + sl);
                    rdw(ks, s, s == 2 ? p2 : p); SBAR();
                }
            sl = sl == 4 * CHB ? 0 : sl + CHB;
        }
#pragma unroll
        for (int ks = 0; ks < 4; ++ks) { acc[2] = mfma(wf[ks][2], xf[ks], acc[2]); SBAR(); }
        BAR();
        ep = *(const LAS u32x2*)(lds + L_CNT + 32);
        const float sqv = ((const LAS float*)(lds + L_XE))[tt * 32 + i];
        float cv[6];
#pragma unroll
        for (int s = 0; s < 6; ++s) cv[s] = ((const LAS float*)(lds + L_CS))[s * 32 + i];
        SBAR();
        acc[0] = mfma(wf[0][0], xf[0], acc[0]); SBAR();
        float lo_, sxv; both32(acc[2][12], lo_, sxv);
        const float mean = sxv * (1.f / D_);
        unsigned mh, ml; split2(-mean, mh, ml);
        u32x4 xe = {h ? 0u : (mh | (mh << 16)), h ? 0u : (ml | (ml << 16)), 0u, 0u};
        PIN(xe); SBAR();
        auto corr = [&](int s) {
            unsigned ch, cl; split2(cv[2 * s] + cv[2 * s + 1], ch, cl);
            const unsigned cw = h ? 0u : (ch | (cl << 16));
            acc[s] = mfma((u32x4){cw, cw, 0u, 0u}, xe, acc[s]); SBAR();
        };
        acc[0] = mfma(wf[1][0], xf[1], acc[0]); SBAR();
        corr(2);
        float rstd = __builtin_amdgcn_rsqf(fmaxf(sqv * (1.f / D_) - mean * mean, 0.f) + EPS);
        PIN(rstd); SBAR();
        u32x4 p0, p1;
        auto fin = [&](int s, int half) {
            const float sc = rstd * __uint_as_float(scl[s]);
            u32x4& p = half ? p1 : p0;
#pragma unroll
            for (int e = 0; e < 4; ++e) p[e] = pk2(acc[s][8 * half + 2 * e] * sc, acc[s][8 * half + 2 * e + 1] * sc);
            if (half && h) { p[2] = pd2[s]; p[3] = pd3[s]; }
            PIN(p); SBAR();
        };
        auto put = [&](int s) {
            if (3 * ct + s >= 32) {
                LAS u32x2* wq = (LAS u32x2*)vtw;
                wq[0] = (u32x2){p0[0], p0[1]}; wq[2] = (u32x2){p0[2], p0[3]}; wq[4] = (u32x2){p1[0], p1[1]}; wq[6] = (u32x2){p1[2], p1[3]};
                const u32x2 t0 = ldstr(vtr), t1 = ldstr(vtr + 288), t2 = ldstr(vtr + 1152), t3 = ldstr(vtr + 1440);
                xf[0] = (u32x4){t0[0], t0[1], t1[0], t1[1]}; wf[0][0] = (u32x4){t2[0], t2[1], t3[0], t3[1]};
            } else { xf[0] = p0; wf[0][0] = p1; }
            SBAR();
        };
        auto sto = [&](int s) { st_c(xf[0], rw, l16, dst[s]); st_c(wf[0][0], rw, l16 + 1024, dst[s]); SBAR(); };
        acc[0] = mfma(wf[2][0], xf[2], acc[0]); SBAR();
        acc[1] = mfma(wf[0][1], xf[0], acc[1]); SBAR();
        fin(2, 0);
        acc[0] = mfma(wf[3][0], xf[3], acc[0]); SBAR();
        corr(0);
        fin(2, 1);
        acc[1] = mfma(wf[1][1], xf[1], acc[1]); SBAR();
        put(2);
        acc[1] = mfma(wf[2][1], xf[2], acc[1]); SBAR();
        fin(0, 0);
        acc[1] = mfma(wf[3][1], xf[3], acc[1]); SBAR();
        corr(1);
        STAMP(2);
        fin(0, 1);
        sto(2); put(0);
        fin(1, 0); fin(1, 1);
        sto(0); put(1); sto(1);
        STAMP(3);
        waitv<0>();
        STAMP(4);
        arrive(lbase + L_CNT + 16, 1, 4, rw, f1o, ep, lane, 3);
    }
    ep[0] = __builtin_amdgcn_readfirstlane(ep[0]); ep[1] = __builtin_amdgcn_readfirstlane(ep[1]);

    const int head = 2 * xcd + (jj & 1), qb = jj >> 1, kh = w >> 2;
    const int r3 = 4 * xcd + (jj & 3), c3 = jj >= 28 ? 6 : (jj >> 2);
    u32x4 a1[28];
    const int w1s = WS_W1 + (8 * c3 + w) * 28 * 1024;
    const int kw = (w + ROTK * c3) & 7;
    f32x16 o;
    {
        const int kvs = WS_KV + head * 64 * 4096 + (kh * 32 + tt) * 4096;
        const unsigned ldst = lbase + w * 4096;
        const int wos = WS_WO + c3 * 65536 + w * 4096;
        const int fro = WS_FR + (lane < 16 ? 16 * c3 + lane : (lane < 32 ? 112 + 16 * c3 + (lane - 16) : ((1568 + 32 * c3 + min(7 * (lane & 7), 31)) * 9363) >> 16)) * 8;
        const int fpo = WS_FP + (lane < 16 ? 8 * (2 * (r3 >> 1) + (lane & 1)) + (lane >> 1) : 8 * (4 * (lane < 23 ? lane - 16 : 0) + (r3 & 3)) + xcd) * 8;
        const u32x4 POISON = {~0u, ~0u, ~0u, ~0u};
        const LAS char* rd = lds + kh * 16384 + l16;
        const LAS char* rd2 = rd + 65536; asm volatile("" : "+v"(rd2));
        u32x4 q0, q1;
        const int odst = WS_O + ((qb * 4 + tt) * 32 + head * 2 + kh) * 1024;
        int po = odst, ph = jj < 28 ? WS_HID + (r3 * 224 + kh * 56 + 8 * c3 + 2 * tt) * 1024 : OOB, pg = jj < 28 ? WS_H + ((r3 * 2 + kh) * 28 + 4 * c3 + tt) * 1024 : OOB;
        int ps = (jj < 28 && w < 2) ? WS_ST + ((r3 * 2 + w) * 14 + 2 * c3) * 256 : OOB;
        bool done = false;
        volatile LAS unsigned* ring = (volatile LAS unsigned*)(lds + L_CNT) + 12;
        unsigned tgt = 0, one = 1; PIN(ring); PIN(one);
#pragma unroll 1
        for (int pass = 0; pass < 2; ++pass) {
            if (pass == 0) {
        if (w >= 6) wait_flags(rw, WS_F1 + (lane < 16 ? (((16 << (lane >> 3)) + head) * 2 + (w & 1)) * 128 + (lane & 7) * 8 : (head * 2 + (qb >> 3)) * 128 + (qb & 7) * 8), ep);
        BAR();
        STAMP(5);
            }
            q0 = ld_c_async(rw, l16, WS_Q + (head * 64 + qb * 4 + tt) * 2048); q1 = ld_c_async(rw, l16, WS_Q + (head * 64 + qb * 4 + tt) * 2048 + 1024);
            dma_c4(rw, l16, kvs, ldst);
            u32x2 frv = ld_flag_async(rw, fro), fpv;
            dma_c4(rw, l16, kvs + 16384, ldst + 32768);
            waitv<4>(); PIN(q0); PIN(q1); PIN(frv); BAR();
            STAMP(6);
            float m;
            if (pass == 0) {
                f32x16 s = mfma(lds128(lds + l16), q0, zero); s = mfma(lds128(lds + 1024 + l16), q1, s);
                m = s[0];
#pragma unroll
                for (int r = 1; r < 16; ++r) m = fmaxf(m, s[r]);
            } else {
                m = -INFINITY;
#pragma unroll 1
                for (int t = 0; t < 64; ++t) {
                    f32x16 s = mfma(ld_c(rw, l16, WS_KV + (head * 64 + t) * 4096), q0, zero); s = mfma(ld_c(rw, l16 + 1024, WS_KV + (head * 64 + t) * 4096), q1, s);
#pragma unroll
                    for (int r = 0; r < 16; ++r) m = fmaxf(m, s[r]);
                }
            }
            {
                const float n = -max32(m); const unsigned t0 = pk2(n, 0.f) & 0xffffu; const float r1 = n - bflo(t0);
                const unsigned t1 = pk2(r1, 0.f) & 0xffffu; const unsigned t2 = pk2(r1 - bflo(t1), 0.f) & 0xffffu;
                if (h) { q1[2] = t0 | (t1 << 16); q1[3] = t2; }
            }
            if (!flag_ok(frv, ep)) wait_flags(rw, fro, ep);
            o = zero;
            f32x16 sA, sB; u32x4 k0, k1, v0, v1;
            sA = mfma(lds128(rd), q0, zero); sA = mfma(lds128(rd + 1024), q1, sA);
            k0 = lds128(rd + 4096); k1 = lds128(rd + 4096 + 1024);
            v0 = lds128(rd + 2048); v1 = lds128(rd + 3072);
#define EX(P, n) P[n] = __builtin_amdgcn_exp2f(P[n])
#define E8(P, n) do { EX(P, n); EX(P, n + 1); EX(P, n + 2); EX(P, n + 3); EX(P, n + 4); EX(P, n + 5); EX(P, n + 6); EX(P, n + 7); PIN(P); SBAR(); } while (0)
#define PRIO(n) do { __builtin_amdgcn_s_setprio(n); SBAR(); } while (0)
#define TPTR(t) (((((t) >> 2) & 2) ? rd2 : rd) + (((t) >> 2) & 1) * 32768 + ((t) & 3) * 4096)
#define ISD(t) ((t) < 2 || (((t) & 2) && (t) < 30))
#define OTH(t) ((t) >= 30 ? 14 + (t) - 30 : ((t) >> 2) * 2 - 2 + ((t) & 1))
#define PN(t) ((t) == 4 ? 4 : (t) == 5 ? 5 : (t) == 8 ? 6 : (t) == 12 ? 0 : (t) == 16 ? 1 : (t) == 20 ? 2 : (t) == 24 ? 3 : -1)
#define WN(t, s) ((s) && PN(t) >= 0 ? -1 : 2 * OTH(t) + (s) - ((t) > 4) - ((t) > 5) - ((t) > 8) - ((t) > 12) - ((t) > 16) - ((t) > 20) - ((t) > 24))
#define SLOT(t, s) do { \
            if (ISD(t)) { constexpr int st_ = (t) < 2 ? 2 : ((t) >> 2) + 3; \
                dma_co<(((t) & 1) * 2 + (s)) * 1024>(rw, l16, st_ < 8 ? kvs + st_ * 16384 : wos + (st_ - 8) * 32768, ldst + (st_ & 3) * 32768); } \
            else if (WN(t, s) < 0) { constexpr int pn_ = PN(t); \
                if (pn_ < 4) st_c(POISON, rw, l16 + (pn_ & 1) * 1024, ph + ((pn_ >> 1) & 1) * 112 * 1024); \
                else st_c(POISON, rw, pn_ == 6 && h ? OOB : l16, pn_ == 4 ? po : (pn_ == 5 ? pg : ps)); } \
            else if (WN(t, s) < NW1) { constexpr int wn_ = ISD(t) || WN(t, s) < 0 || WN(t, s) >= NW1 ? 0 : WN(t, s); a1[wn_] = ld_c(rw, l16 + (wn_ & 3) * 1024, w1s + (wn_ >> 2) * 4096); } \
            SBAR(); } while (0)
#define STEP(SC, SN, t) do { \
            constexpr bool arr_ = ((t) & 3) == 1 && (t) < 29, chk_ = ((t) & 3) == 2 && (t) < 30; \
            unsigned cv_; \
            if (USEBAR && chk_ && kh) { waitv<(t) == 2 ? 4 : ((t) == 6 ? 8 : 12)>(); BAR(); } \
            PRIO(0); E8(SC, 0); PRIO(1); if (chk_ && !USEBAR) { cv_ = *ring; SBAR(); } E8(SC, 8); PRIO(2); \
            if (USEBAR && chk_ && !kh) { waitv<(t) == 2 ? 4 : ((t) == 6 ? 8 : 12)>(); BAR(); } \
            if (arr_ && !USEBAR) { waitv<(t) == 1 ? 2 : ((t) == 5 ? 6 : 10)>(); asm volatile("s_mov_b64 exec, 1\n\tds_add_u32 %0, %1\n\ts_mov_b64 exec, -1" :: "v"(ring), "v"(one) : "memory"); SBAR(); } \
            if (chk_ && !USEBAR) { tgt += 8; if (__builtin_amdgcn_readfirstlane(cv_) < tgt) spin_lds(ring, tgt); asm volatile("" ::: "memory"); SBAR(); } \
            if ((t) == 18) { if (w == 0 && lane == 0) st_flag(rw, WS_FP + b * 8, ep); SBAR(); } \
            if ((t) < 31) { SN = mfma(k0, q0, zero); SBAR(); } \
            SLOT(t, 0); \
            if ((t) < 31) { SN = mfma(k1, q1, SN); SBAR(); } \
            SLOT(t, 1); \
            if ((t) == 21 && WARM2) { asm volatile("s_mov_b32 m0, %0\n\ts_nop 3\n\tbuffer_load_dword %1, %2, %3 offen sc1 lds" :: "s"(lbase + L_DUMP), "v"(lane < 49 ? lane * 128 : OOB), "s"(rw), "s"(WS_W2 + (jj * 8 + w) * 49 * 128) : "memory"); SBAR(); } \
            if ((t) == 30) { fpv = ld_flag_async(rw, fpo); SBAR(); } \
            o = pv(v0, SC, 0, o); SBAR(); \
            if ((t) < 30) { k0 = lds128(TPTR((t) + 2)); k1 = lds128(TPTR((t) + 2) + 1024); SBAR(); } \
            if ((t) < 31) { v0 = lds128(TPTR((t) + 1) + 2048); SBAR(); } \
            o = pv(v1, SC, 1, o); SBAR(); if ((t) < 31) { v1 = lds128(TPTR((t) + 1) + 3072); SBAR(); } \
            } while (0)
#define STEP4(t) STEP(sA, sB, t); STEP(sB, sA, (t) + 1); STEP(sA, sB, (t) + 2); STEP(sB, sA, (t) + 3)
            CSTAMP(22); STAMP(23);
            STEP4(0); STEP4(4); STEP4(8); STEP4(12); STEP4(16); STEP4(20); STEP4(24); STEP4(28);
            __builtin_amdgcn_s_setprio(0);
            STAMP(7); CSTAMP(24);
            LAS f32x4* red = (LAS f32x4*)(lds + L_RED) + lane;
            LAS float* lsum = (LAS float*)(lds + L_FLAG + 64);
            f32x8 mine;
            if (kh) {
                red[w * 128] = __builtin_shufflevector(o, o, 0, 1, 2, 3); red[w * 128 + 64] = __builtin_shufflevector(o, o, 4, 5, 6, 7);
                mine = __builtin_shufflevector(o, o, 8, 9, 10, 11, 12, 13, 14, 15);
            } else {
                red[w * 128] = __builtin_shufflevector(o, o, 8, 9, 10, 11); red[w * 128 + 64] = __builtin_shufflevector(o, o, 12, 13, 14, 15);
                mine = __builtin_shufflevector(o, o, 0, 1, 2, 3, 4, 5, 6, 7);
            }
            lsum[w * 64 + lane] = o[12];
            BARRIER();
            const float l = lsum[tt * 64 + 32 + i] + lsum[(tt + 4) * 64 + 32 + i];
            const f32x4 t0 = red[(w ^ 4) * 128], t1 = red[(w ^ 4) * 128 + 64];
            const float rl = __builtin_amdgcn_rcpf(l);
            mine += __builtin_shufflevector(t0, t1, 0, 1, 2, 3, 4, 5, 6, 7);
            float amax = l;
#pragma unroll
            for (int r = 0; r < 8; ++r) amax = fmaxf(amax, fabsf(mine[r]));
            const bool bad = pass == 0 && __any(!(amax <= 1e30f && l == l));
            st_c((u32x4){pk2(mine[0] * rl, mine[1] * rl), pk2(mine[2] * rl, mine[3] * rl), pk2(mine[4] * rl, mine[5] * rl), pk2(mine[6] * rl, mine[7] * rl)},
                 rw, l16, (bad || done) ? OOB : odst);
#pragma unroll
            for (int k = 0; k < NW1; ++k) PIN(a1[k]);
            done = !bad;
            if (bad) ((volatile LAS unsigned*)(lds + L_CNT))[6] = 1u;
            waitv<1>(); PIN(fpv);
            if (!flag_ok(fpv, ep)) wait_flags(rw, fpo, ep);
            BARRIER();
            if (__builtin_amdgcn_readfirstlane(((volatile LAS unsigned*)(lds + L_CNT))[6]) == 0u) break;
            waitv<0>();
            po = ph = pg = ps = OOB;
        }
    }
    STAMP(8);
    if (jj >= 28) {
        if (b == 255 && w == 0) {
#pragma unroll 1
            for (int n = 0; n < 4; ++n) wait_flags(rw, WS_FR + (n * 64 + lane) * 8, ep);
            if (lane == 0) st_flag(rw, WS_EPOCH, ep);
        }
        STAMP_OUT();
        return;
    }

    float cs1 = 0.f;
    unsigned bias, b2v;
    u32x4 a2[28];
    const int w2s = WS_W2 + (2 * c3 * 112 + 7 * kw) * 1024;
#define W2O(q) ((((q) & 1) * 112 + ((q) >= 14 ? 56 - 7 : 0) + ((q) >> 1)) * 1024)
    {
        const int nt2 = w & 1, rt = (w >> 1) & 1;
        f32x16 acc;
        u32x4 xr[4];
        auto ldw = [&](int k) {
            if (k < 4) a1[24 + k] = ld_c(rw, l16 + k * 1024, w1s + 6 * 4096);
            else a2[k - 4] = ld_c(rw, l16, w2s + W2O(k - 4));
        };
        auto fetch = [&]() {
            dma_c4(rw, l16, WS_O + r3 * 65536 + w * 8192, lbase + 65536 + w * 8192);
            dma_c4(rw, l16, WS_O + r3 * 65536 + w * 8192 + 4096, lbase + 65536 + w * 8192 + 4096);
        };
        float cs2 = 0.f;
        STAMP(9);
        fetch();
        {
            const rsrc_t rx2 = __builtin_amdgcn_make_buffer_rsrc((void*)x, 0, w < 4 ? 2048 * 896 : 0, 0x00020000);
#pragma unroll
            for (int g = 0; g < 4; ++g) xr[g] = __builtin_amdgcn_raw_buffer_load_b128(rx2, i * 896 + 16 * h + 32 * g, (r3 * 64 + rt * 32) * 896 + c3 * 128, 0);
        }
        bias = b1[(4 * c3 + (w >> 1)) * 64 + 2 * i + (w & 1)];
        b2v = *(const unsigned*)(b2 + c3 * 64 + 2 * i);
        asm volatile("" ::: "memory"); SBAR();
#pragma unroll
        for (int k = 0; k < NE2; ++k) ldw(k);
        SBAR();
#pragma unroll
        for (int k = 0; k < 24; ++k) { cs1 = dot2(a1[k][0], ONES, cs1); cs2 = dot2(a1[k][1], ONES, cs2); cs1 = dot2(a1[k][2], ONES, cs1); cs2 = dot2(a1[k][3], ONES, cs2); }
        PIN(cs1); PIN(cs2);
        waitv<NE2>();
#pragma unroll
        for (int g = 0; g < 4; ++g)
#pragma unroll
            for (int e = 0; e < 4; ++e) acc[4 * g + e] = nt2 ? bfhi(xr[g][e]) : bflo(xr[g][e]);
        PIN(acc); PIN(bias);
        if (w == 6) ((LAS unsigned*)(lds + L_B2))[i] = b2v;
#pragma unroll 1
        for (int tries = 0; tries < 4096; ++tries) {
            unsigned m = 0;
#pragma unroll
            for (int p = 0; p < 8; ++p) m = pmax(m, lds128(lds + 65536 + w * 8192 + p * 1024 + l16));
            if (!poisoned(m)) break;
            fetch();
            asm volatile("s_waitcnt vmcnt(0)" ::: "memory");
        }
        BAR();
        STAMP(10);
        if (w < 4) {
            const LAS char* pa = lds + nt2 * 32768 + l16; const LAS char* pb = lds + 65536 + rt * 32768 + l16;
            u32x4 fa[4], fb[4];
#pragma unroll
            for (int k = 0; k < 4; ++k) { fa[k] = lds128(pa + k * 1024); fb[k] = lds128(pb + k * 1024); }
            SBAR();
#pragma unroll
            for (int k = 0; k < 32; ++k) {
                acc = mfma(fa[k & 3], fb[k & 3], acc); SBAR();
                if (k < 28) { fa[k & 3] = lds128(pa + (k + 4) * 1024); fb[k & 3] = lds128(pb + (k + 4) * 1024); SBAR(); }
                if (k % 3 == 0 && k < 30 && NE2 + k / 3 < 22) { ldw(NE2 + k / 3); SBAR(); }
            }
            BAR();
            const int rtile = r3 * 2 + rt, nt = 2 * c3 + nt2;
            const u32x4 p0 = pack8(acc, 0), p1 = pack8(acc, 1);
            st_c(p0, rw, l16, WS_H + (rtile * 28 + 2 * nt) * 1024); st_c(p1, rw, l16 + 1024, WS_H + (rtile * 28 + 2 * nt) * 1024);
            float s = 0.f, q = 0.f, s2 = 0.f, q2 = 0.f;
#pragma unroll
            for (int e = 0; e < 4; ++e) { s = dot2(p0[e], ONES, s); q = dot2(p0[e], p0[e], q); s2 = dot2(p1[e], ONES, s2); q2 = dot2(p1[e], p1[e], q2); }
            s = sum32(s + s2); q = sum32(q + q2);
            __builtin_amdgcn_raw_buffer_store_b32(__float_as_uint(h ? q : s), rw, lane * 4, WS_ST + (rtile * 14 + nt) * 256, 16);
            *(LAS u32x4*)(lds + L_HS + w * 2048 + l16) = p0; *(LAS u32x4*)(lds + L_HS + w * 2048 + 1024 + l16) = p1;
            STAMP(11);
        } else {
#pragma unroll
            for (int k = NE2; k < 22; ++k) ldw(k);
            BAR(); __builtin_amdgcn_s_sleep(E3SLEEP); }
#pragma unroll
        for (int k = 24; k < 28; ++k) { cs1 = dot2(a1[k][0], ONES, cs1); cs2 = dot2(a1[k][1], ONES, cs2); cs1 = dot2(a1[k][2], ONES, cs1); cs2 = dot2(a1[k][3], ONES, cs2); }
        cs1 = sum32(cs1 + cs2);
    }
    u32x4 bf[14];
    int ha[4], hb[4];
#pragma unroll
    for (int q = 0; q < 4; ++q) { ha[q] = WS_HID + (r3 * 224 + 7 * kw + (q >> 1) * 56 + (q & 1) * 4) * 1024; hb[q] = ha[q] + 112 * 1024; }
#define LDH(k, so) asm volatile("buffer_load_dwordx4 %0, %1, %2, %3 offen offset:%4 sc1" : "=v"(bf[k]) : "v"(l16), "s"(rw), "s"(so[((k) / 7) * 2 + (((k) % 7) >> 2)]), "n"((((k) % 7) & 3) * 1024) : "memory");
#define FOR7(M, g, so) M(7 * g, so) M(7 * g + 1, so) M(7 * g + 2, so) M(7 * g + 3, so) M(7 * g + 4, so) M(7 * g + 5, so) M(7 * g + 6, so)
    {
        const int ft = 8 * c3 + w;
        unsigned ch, cl; split2(cs1, ch, cl);
        u32x4 ae = {h ? 0u : (ch | (cl << 16)), h ? 0u : (ch | (cl << 16)), h ? 0u : (bias | (bias << 16)), 0u};
        PIN(ae);
        STAMP(12);
#pragma unroll 1
        for (int tries = 0; tries < 4096; ++tries) {
            dma_c4(rw, l16, WS_H + r3 * 57344 + w * 7168, lbase + w * 7168);
#pragma unroll
            for (int p = 4; p < 7; ++p) dma_c(rw, l16, WS_H + r3 * 57344 + w * 7168 + p * 1024, lbase + w * 7168 + p * 1024);
            dma_c(rw, l16, w < 7 ? WS_ST + r3 * 7168 + w * 1024 : OOB, lbase + 57344 + w * 1024);
            waitv<0>();
            unsigned m = pmax(0u, lds128(lds + 57344 + w * 1024 + l16));
#pragma unroll
            for (int p = 0; p < 7; ++p) m = pmax(m, lds128(lds + w * 7168 + p * 1024 + l16));
                        if (!poisoned(m)) break;
        }
        BARRIER();
        auto park = [&](int q) { dma_c(rw, l16, w2s + W2O(q), lbase + 65536 + w * 10240 + (q - 18) * 1024); };
        if (w >= 4) {
#pragma unroll
            for (int q = 18; q < 28; ++q) park(q);
        }
        STAMP(13); CSTAMP(29);
        u32x4 fb[4];
#pragma unroll
        for (int k = 0; k < 4; ++k) fb[k] = lds128(lds + k * 1024 + l16);
#pragma unroll
        for (int rt = 0; rt < 2; ++rt) {
            f32x16 acc = zero;
            const LAS float* sp = (const LAS float*)(lds + 57344) + rt * 14 * 64 + lane;
            float st = 0.f, sv[14];
            u32x4 be; float g1, g2, sd, mean, var, rs;
            if (rt) __builtin_amdgcn_s_setprio(2); else __builtin_amdgcn_s_setprio(3);
            SBAR();
#pragma unroll
            for (int k = 0; k < 28; ++k) {
                acc = mfma(a1[k], fb[k & 3], acc); SBAR();
                if (rt * 28 + k + 4 < 56) fb[k & 3] = lds128(lds + (rt * 28 + k + 4) * 1024 + l16);
                if (k < 7) { sv[2 * k] = sp[2 * k * 64]; sv[2 * k + 1] = sp[(2 * k + 1) * 64]; }
                if (k >= 4 && k < 11) { st += sv[2 * (k - 4)] + sv[2 * (k - 4) + 1]; PIN(st); }
                if (k == 12) { both32(st, mean, var); mean *= 1.f / D_; PIN(mean); PIN(var); }
                if (k == 13) { var = fmaxf(var * (1.f / D_) - mean * mean, 0.f); rs = __builtin_amdgcn_rsqf(var + EPS); PIN(var); PIN(rs); }
                if (k == 14) { var = __builtin_amdgcn_rsqf(var * rs * rs + EPS); PIN(var); }
                if (k == 15) { rs *= var; sd = __builtin_amdgcn_rcpf(rs); g1 = -2.3022081981f * rs; PIN(rs); PIN(sd); PIN(g1); }
                if (k == 16) { g2 = -0.1029432397f * rs * rs * rs; be[0] = pk2(-mean, -mean); PIN(g2); PIN(be[0]); }
                if (k == 17) { const float r_ = -mean - bflo(be[0]); be[1] = pk2(r_, r_); be[0] = h ? 0u : be[0]; PIN(be[0]); PIN(be[1]); }
                if (k == 18) { be[2] = pk2(sd, sd); be[1] = h ? 0u : be[1]; PIN(be[1]); PIN(be[2]); }
                if (k == 19) { be[2] = pk2(sd, sd - bflo(be[2])); be[2] = h ? 0u : be[2]; be[3] = 0u; PIN(be); }
                if (k == 21) acc = mfma(ae, be, acc);
                if (rt == 0 && w < 4 && k % 3 != 2) { const int q = NE2 + 6 + k - k / 3; if (q < 18) a2[q] = ld_c(rw, l16, w2s + W2O(q)); else if (q < 28) park(q); }
                if (rt == 1 && k == 13) waitv<2>();
                if (rt == 1 && k >= 14 && k < 24) a2[k + 4] = lds128(lds + 65536 + w * 10240 + (k - 14) * 1024 + l16);
                SBAR();
            }
            if (rt) __builtin_amdgcn_s_setprio(0); else __builtin_amdgcn_s_setprio(1);
            if (rt) {
#pragma unroll
                for (int q = 0; q < 28; ++q) PIN(a2[q]);
                asm volatile("s_nop 4" : "+s"(ha[0]), "+s"(ha[1]), "+s"(ha[2]), "+s"(ha[3]), "+s"(hb[0]), "+s"(hb[1]), "+s"(hb[2]), "+s"(hb[3]));
                STAMP(16);
            }
            SBAR();
#pragma unroll
            for (int hf = 0; hf < 2; ++hf) {
                const f32x8 a = hf ? __builtin_shufflevector(acc, acc, 8, 9, 10, 11, 12, 13, 14, 15) : __builtin_shufflevector(acc, acc, 0, 1, 2, 3, 4, 5, 6, 7);
                f32x8 u;
#define ST8(EXPR) _Pragma("unroll") for (int j = 0; j < 8; ++j) { float t_ = (EXPR); asm volatile("" : "+v"(t_)); u[j] = t_; } SBAR();
#define LA(n) if (rt) { if (hf) { LDH(7 + n, ha) } else { LDH(n, ha) } SBAR(); }
                LA(0) ST8(a[j] * a[j]) LA(1) ST8(__builtin_fmaf(u[j], g2, g1)) LA(2) ST8(u[j] * a[j]) LA(3) ST8(__builtin_amdgcn_exp2f(u[j]))
                LA(4) ST8(__builtin_fmaf(u[j], sd, sd)) LA(5) ST8(__builtin_amdgcn_rcpf(u[j])) LA(6) ST8(u[j] * a[j])
#pragma unroll
                for (int j = 0; j < 8; ++j) acc[8 * hf + j] = u[j];
            }
            const int dst = WS_HID + ((r3 * 2 + rt) * 112 + kh * 56 + 8 * c3 + 2 * tt) * 1024;
            u32x4 g0 = pack8(acc, 0), g8 = pack8(acc, 1);
            st_c(g0, rw, l16, dst); st_c(g8, rw, l16 + 1024, dst);
            SBAR();
            if (rt == 0) STAMP(14);
        }
        STAMP(15); CSTAMP(30);
    }
    {
        volatile LAS unsigned* cnt = (volatile LAS unsigned*)(lds + L_CNT);
        auto put = [&](const f32x16& acc0, const f32x16& acc1, int rt) {
            LAS f32x2* red = (LAS f32x2*)(lds + (rt ? 0 : 65536) + w * 1024 + l16);
#pragma unroll
            for (int p = 0; p < 8; ++p) { red[p * 1024] = (f32x2){acc0[2 * p], acc0[2 * p + 1]}; red[p * 1024 + 1] = (f32x2){acc1[2 * p], acc1[2 * p + 1]}; }
            SBAR();
        };
        auto fin = [&](int rt, int p) {
            const LAS f32x4* red = (const LAS f32x4*)(lds + (rt ? 0 : 65536) + p * 8192) + lane;
            const int cl = c3 * 64 + 2 * ((2 * p & 3) + 8 * (p >> 1) + 4 * h);
            const u32x2 bb = *(const LAS u32x2*)(lds + L_B2 + (cl - c3 * 64) * 2);
            const LAS char* hp = lds + L_HS + rt * 4096 + (p >> 2) * 1024 + l16 + (p & 3) * 4;
            const unsigned h0 = *(const LAS unsigned*)hp, h1 = *(const LAS unsigned*)(hp + 2048);
            const f32x4 r = ((red[0] + red[64]) + (red[128] + red[192])) + ((red[256] + red[320]) + (red[384] + red[448]));
            const u32x2 res = {pk2(r[0] + bflo(bb[0]) + bflo(h0), r[2] + bfhi(bb[0]) + bflo(h1)), pk2(r[1] + bflo(bb[1]) + bfhi(h0), r[3] + bfhi(bb[1]) + bfhi(h1))};
            *(u32x2*)(out + (size_t)(r3 * 64 + rt * 32 + i) * D_ + cl) = res;
        };
        asm volatile("s_mov_b64 exec, 1\n\tds_add_u32 %0, %1\n\ts_mov_b64 exec, -1" :: "v"(lbase + L_CNT + 40), "v"(1u) : "memory");
#define WAIT7(g, n) asm volatile("s_waitcnt vmcnt(" #n ")" : "+v"(bf[7 * g]), "+v"(bf[7 * g + 1]), "+v"(bf[7 * g + 2]), "+v"(bf[7 * g + 3]), "+v"(bf[7 * g + 4]), "+v"(bf[7 * g + 5]), "+v"(bf[7 * g + 6]) :: "memory")
#define CHECK7(g, n, so, id) do { WAIT7(g, n); _Pragma("unroll 1") for (int tries = 0; tries < 4096; ++tries) { unsigned m = 0; \
            _Pragma("unroll") for (int k = 7 * g; k < 7 * g + 7; ++k) m = pmax(m, bf[k]); \
            if (!poisoned(m)) break; \
            TRY(id); FOR7(LDH, g, so) WAIT7(g, 0); } } while (0)
#define MM(k, so) acc0 = mfma(a2[2 * (k)], bf[k], acc0); acc1 = mfma(a2[2 * (k) + 1], bf[k], acc1); SBAR();
#define MML(k, so) MM(k, so) LDH((k) - 7, so) SBAR();
        f32x16 acc0 = zero, acc1 = zero;
        CHECK7(0, 9, ha, 25);
        STAMP(17);
        FOR7(MM, 0, hb)
        CHECK7(1, 2, ha, 26);
        FOR7(MML, 1, hb)
        STAMP(18);
        spin_lds(cnt + 10, 8);
        if (w < 4) { put(acc0, acc1, 0); __builtin_amdgcn_s_sleep(B1SLEEP); }
        FOR7(LDH, 1, hb)
        if (w >= 4) put(acc0, acc1, 0);
        asm volatile("s_waitcnt lgkmcnt(0)\n\ts_mov_b64 exec, 1\n\tds_add_u32 %0, %1\n\ts_mov_b64 exec, -1" :: "v"(lbase + L_CNT + 44), "v"(1u) : "memory");
        acc0 = zero; acc1 = zero;
        CHECK7(0, 7, hb, 27);
        STAMP(19);
        FOR7(MM, 0, hb)
        CHECK7(1, 0, hb, 28);
        FOR7(MM, 1, hb)
        STAMP(20);
        put(acc0, acc1, 1);
        if (w < 4) {
#pragma unroll 1
            for (int spin = 0; spin < (1 << 24) && cnt[11] != 8u; ++spin) __builtin_amdgcn_s_sleep(1);
            asm volatile("" ::: "memory");
            fin(0, w); fin(0, w + 4);
        }
        BARRIER();
        fin(1, w);
    }
    STAMP(21);
    STAMP_OUT();
    asm volatile("88:");
}

extern "C" void kernel_launch(void* const* d_in, const int* in_sizes, int n_in, void* d_out, int out_size, void* d_ws, size_t ws_size, hipStream_t stream) {
    (void)hipFuncSetAttribute((const void*)fused_block, hipFuncAttributeMaxDynamicSharedMemorySize, LDS_BYTES);
    fused_block<<<256, 512, LDS_BYTES, stream>>>((const u16*)d_in[0], (const u16*)d_in[1], (const u16*)d_in[2], (const u16*)d_in[4], (const u16*)d_in[5],
                                                 (const u16*)d_in[6], (const u16*)d_in[7], (u16*)d_out, (char*)d_ws);
}
```

```cpp
#include <hip/hip_runtime.h>
#include <stdint.h>

typedef unsigned short u16;
typedef unsigned long long u64;
typedef __bf16 bf16x2 __attribute__((ext_vector_type(2)));
typedef __bf16 bf16x8 __attribute__((ext_vector_type(8)));
typedef __bf16 bf16x16 __attribute__((ext_vector_type(16)));
typedef float f32x2 __attribute__((ext_vector_type(2)));
typedef float f32x4 __attribute__((ext_vector_type(4)));
typedef float f32x8 __attribute__((ext_vector_type(8)));
typedef float f32x16 __attribute__((ext_vector_type(16)));
typedef unsigned u32x2 __attribute__((ext_vector_type(2)));
typedef unsigned u32x4 __attribute__((ext_vector_type(4)));
typedef short s16x4 __attribute__((ext_vector_type(4)));
typedef __amdgpu_buffer_rsrc_t rsrc_t;
#define LAS __attribute__((address_space(3)))
#define DI __device__ __forceinline__
#define SBAR() __builtin_amdgcn_sched_barrier(0)
#define PIN(x) asm volatile("" : "+v"(x))

#define USEBAR 0
#define NW1 24
#define NE2 4
#define B1SLEEP 4
#define WARM2 0
#define NP3A 6
#define POLLW 7
#define LATE0 0
#define PRET 3
#define TSTRIDE (1 << 20)
#define OUTAUX 16
#define ROTK 1
#define E3SLEEP 8
#define W1MODE 1
constexpr int D_ = 448, F_ = 1792;
constexpr float EPS = 1e-5f;
constexpr int WS_EPOCH = 0;
constexpr int WS_F1 = 1024;
constexpr int WS_FR = WS_F1 + 96 * 128;
constexpr int WS_FP = WS_FR + 2048;
constexpr int WS_ST = 32768;
constexpr int WS_Q = WS_ST + 64 * 14 * 256;
constexpr int WS_KV = WS_Q + 16 * 64 * 2048;
constexpr int WS_O = WS_KV + 16 * 64 * 4096;
constexpr int WS_H = WS_O + 64 * 32 * 1024;
constexpr int WS_W1 = WS_H + 64 * 28 * 1024;
constexpr int WS_W2 = WS_W1 + 56 * 28 * 1024;
constexpr int WS_WO = WS_W2 + 14 * 112 * 1024;
constexpr int WS_HID = WS_WO + 14 * 32 * 1024;
constexpr int WS_END = WS_HID + 64 * 112 * 1024;
static_assert(WS_FP + 2048 <= WS_ST, "flags overlap");
constexpr int OOB = 0x40000000;

constexpr int XCH = 16384, WCH = 12288, CHB = XCH + WCH;
constexpr int L_MISC = 5 * CHB;
constexpr int L_CS = L_MISC, L_XE = L_MISC + 3072, L_CW = L_XE + 4096, L_RT = L_CW + 768;
constexpr int L_VT = 2 * CHB;
constexpr int L_RED = 131072;
constexpr int L_FLAG = L_RED + 16384;
constexpr int L_HS = L_RED + 16384 + 64 + 2048;
constexpr int L_B2 = L_HS + 8192, L_B1 = L_B2 + 1024;
constexpr int L_CNT = 163840 - 1024 - 64;
constexpr int L_DUMP = 163840 - 1024;
constexpr int LDS_BYTES = 163840;

#define STAMP_DECL
#define STAMP(i)
#define CSTAMP(i)
#define TRY(i)
#define STAMP_OUT()

DI f32x16 mfma(u32x4 a, u32x4 b, f32x16 c) {
    return __builtin_amdgcn_mfma_f32_32x32x16_bf16(__builtin_bit_cast(bf16x8, a), __builtin_bit_cast(bf16x8, b), c, 0, 0, 0);
}
DI f32x4 mfma16(u32x4 a, u32x4 b, f32x4 c) {
    return __builtin_amdgcn_mfma_f32_16x16x32_bf16(__builtin_bit_cast(bf16x8, a), __builtin_bit_cast(bf16x8, b), c, 0, 0, 0);
}
DI f32x4 mfma4(unsigned a0, unsigned a1, unsigned b0, unsigned b1, f32x4 c) {
    return __builtin_amdgcn_mfma_f32_4x4x4bf16_1k(__builtin_bit_cast(s16x4, (u32x2){a0, a1}), __builtin_bit_cast(s16x4, (u32x2){b0, b1}), c, 0, 0, 0);
}
DI f32x16 pv(u32x4 v, const f32x16& p, int s, f32x16 o) {
    f32x8 ph = {p[8 * s], p[8 * s + 1], p[8 * s + 2], p[8 * s + 3], p[8 * s + 4], p[8 * s + 5], p[8 * s + 6], p[8 * s + 7]};
    return __builtin_amdgcn_smfmac_f32_32x32x32_bf16(__builtin_bit_cast(bf16x8, v), __builtin_bit_cast(bf16x16, ph), o, 0xDDDD, 0, 0);
}
DI unsigned pk2(float lo, float hi) { f32x2 v = {lo, hi}; return __builtin_bit_cast(unsigned, __builtin_convertvector(v, bf16x2)); }
DI float bflo(unsigned u) { return __uint_as_float(u << 16); }
DI float bfhi(unsigned u) { return __uint_as_float(u & 0xffff0000u); }
DI float dot2(unsigned a, unsigned b, float c) { return __builtin_amdgcn_fdot2_f32_bf16(__builtin_bit_cast(bf16x2, a), __builtin_bit_cast(bf16x2, b), c, false); }
DI void both32(float v, float& lo, float& hi) {
    auto r = __builtin_amdgcn_permlane32_swap(__float_as_uint(v), __float_as_uint(v), false, false);
    lo = __uint_as_float(r[0]); hi = __uint_as_float(r[1]);
}
DI float sum32(float v) { float a, b; both32(v, a, b); return a + b; }
DI float max32(float v) { float a, b; both32(v, a, b); return fmaxf(a, b); }
DI u32x4 lds128(const LAS char* p) { return *(const LAS u32x4*)p; }
DI u32x2 ldstr(const LAS char* p) { return __builtin_bit_cast(u32x2, __builtin_amdgcn_ds_read_tr16_b64_v4i16((LAS s16x4*)p)); }
DI void split2(float v, unsigned& hi, unsigned& lo) { hi = pk2(v, 0.f) & 0xffffu; lo = pk2(v - bflo(hi), 0.f) & 0xffffu; }
DI u32x4 pack8(const f32x16& a, int s) {
    u32x4 r;
    r[0] = pk2(a[8 * s + 0], a[8 * s + 1]); r[1] = pk2(a[8 * s + 2], a[8 * s + 3]);
    r[2] = pk2(a[8 * s + 4], a[8 * s + 5]); r[3] = pk2(a[8 * s + 6], a[8 * s + 7]);
    return r;
}
DI void dma(rsrc_t rs, int voff, int soff, unsigned lds_addr) {
    asm volatile("s_mov_b32 m0, %0\n\ts_nop 3\n\tbuffer_load_dwordx4 %1, %2, %3 offen lds" :: "s"(lds_addr), "v"(voff), "s"(rs), "s"(soff) : "memory");
}
DI void dma_c(rsrc_t rs, int voff, int soff, unsigned lds_addr) {
    asm volatile("s_mov_b32 m0, %0\n\ts_nop 3\n\tbuffer_load_dwordx4 %1, %2, %3 offen sc1 lds" :: "s"(lds_addr), "v"(voff), "s"(rs), "s"(soff) : "memory");
}
DI void dma_c4(rsrc_t rs, int voff, int soff, unsigned lds_addr) {
    asm volatile("s_mov_b32 m0, %0\n\ts_nop 3\n\tbuffer_load_dwordx4 %1, %2, %3 offen sc1 lds\n\tbuffer_load_dwordx4 %1, %2, %3 offen offset:1024 sc1 lds\n\t"
                 "buffer_load_dwordx4 %1, %2, %3 offen offset:2048 sc1 lds\n\tbuffer_load_dwordx4 %1, %2, %3 offen offset:3072 sc1 lds"
                 :: "s"(lds_addr), "v"(voff), "s"(rs), "s"(soff) : "memory");
}
DI u32x4 ld_c(rsrc_t rs, int voff, int soff) { return __builtin_amdgcn_raw_buffer_load_b128(rs, voff, soff, 16); }
DI void st_c(u32x4 v, rsrc_t rs, int voff, int soff) { __builtin_amdgcn_raw_buffer_store_b128(v, rs, voff, soff, 16); }
DI u32x2 ld_flag(rsrc_t rs, int voff) { asm volatile("" ::: "memory"); return __builtin_amdgcn_raw_buffer_load_b64(rs, voff, 0, 16); }
DI void st_flag(rsrc_t rs, int voff, u32x2 e) { __builtin_amdgcn_raw_buffer_store_b64(e, rs, voff, 0, 16); }
template <int N> DI void waitv() {
    asm volatile("s_waitcnt vmcnt(%0)" :: "n"(N) : "memory");
    if (N == 0) __builtin_amdgcn_s_waitcnt(0x0F70);
}
#define BAR() asm volatile("s_barrier" ::: "memory")
#define BARRIER() asm volatile("s_waitcnt lgkmcnt(0)\n\ts_barrier" ::: "memory")
DI bool flag_ok(u32x2 f, u32x2 e) { return __all(f[0] == e[0] && f[1] == e[1]); }
DI void wait_flags(rsrc_t rs, int voff, u32x2 e) {
#pragma unroll 1
    for (int spin = 0; spin < (1 << 18); ++spin) {
        if (flag_ok(ld_flag(rs, voff), e)) break;
        __builtin_amdgcn_s_sleep(1);
    }
    asm volatile("" ::: "memory");
}
DI void arrive(unsigned cnt_addr, unsigned inc, unsigned n, rsrc_t rs, int foff, u32x2 e, int lane, int nl = 1) {
    unsigned old;
    asm volatile("s_mov_b64 exec, 1\n\tds_add_rtn_u32 %0, %1, %2\n\ts_mov_b64 exec, -1\n\ts_waitcnt lgkmcnt(0)" : "=&v"(old) : "v"(cnt_addr), "v"(inc) : "memory");
    if (__builtin_amdgcn_readfirstlane(old) + inc == n && lane < nl) st_flag(rs, foff, e);
}
template <int OFF> DI void dma_co(rsrc_t rs, int voff, int soff, unsigned lds_addr) {
    asm volatile("s_mov_b32 m0, %0\n\ts_nop 3\n\tbuffer_load_dwordx4 %1, %2, %3 offen offset:%4 sc1 lds" :: "s"(lds_addr), "v"(voff), "s"(rs), "s"(soff), "n"(OFF) : "memory");
}
DI u32x4 ld_c_async(rsrc_t rs, int voff, int soff) { u32x4 r; asm volatile("s_nop 4\n\tbuffer_load_dwordx4 %0, %1, %2, %3 offen sc1" : "=v"(r) : "v"(voff), "s"(rs), "s"(soff) : "memory"); return r; }
DI u32x2 ld_flag_async(rsrc_t rs, int voff) { u32x2 r; asm volatile("buffer_load_dwordx2 %0, %1, %2, 0 offen sc1" : "=v"(r) : "v"(voff), "s"(rs) : "memory"); return r; }
DI unsigned pmax(unsigned m, u32x4 v) { return max(max(max(max(v[0], v[1]), m), v[2]), v[3]); }
DI bool poisoned(unsigned m) { return __any(m == 0xffffffffu); }
DI void spin_lds(volatile LAS unsigned* p, unsigned tgt) {
#pragma unroll 1
    for (int spin = 0; spin < (1 << 24); ++spin) { if (__builtin_amdgcn_readfirstlane(*p) >= tgt) break; __builtin_amdgcn_s_sleep(1); }
}
DI float gelu_tanh(float x) {
    const float t = __builtin_fmaf(x * x, -0.1029432397f, -2.3022081981f);
    const float e = __builtin_amdgcn_exp2f(x * t);
    return x * __builtin_amdgcn_rcpf(1.f + e);
}

__global__ __launch_bounds__(512, 2) void fused_block(const u16* __restrict__ x, const u16* __restrict__ wqkv, const u16* __restrict__ wo,
                                                       const u16* __restrict__ w1, const u16* __restrict__ b1, const u16* __restrict__ w2,
                                                       const u16* __restrict__ b2, u16* __restrict__ out, char* __restrict__ ws) {
    extern __shared__ __attribute__((aligned(16))) char smem[];
    LAS char* lds = (LAS char*)smem;
    const unsigned lbase = (unsigned)(uintptr_t)lds;
    const int lane = threadIdx.x & 63, w = __builtin_amdgcn_readfirstlane(threadIdx.x >> 6);
    const int i = lane & 31, h = lane >> 5, l16 = lane * 16;
    const int b = blockIdx.x, xcd = b & 7, jj = b >> 3;
    STAMP_DECL; STAMP(0);
    const int rb = 4 * (xcd >> 1) + (jj & 3), ct = 8 * (xcd & 1) + (jj >> 2);
    const int tt = w & 3;
        const rsrc_t rx = __builtin_amdgcn_make_buffer_rsrc((void*)x, 0, 2048 * 896, 0x00020000);
        const rsrc_t rq = __builtin_amdgcn_make_buffer_rsrc((void*)wqkv, 0, 448 * 2688, 0x00020000);
        const int vx0 = (lane >> 3) * 896 + (((lane & 7) ^ (lane >> 4)) << 4), vx1 = vx0 ^ 64;
        int vw[3]; u64 mk[3];
#pragma unroll
        for (int p = 0; p < 3; ++p) { const int q = (3 * tt + p) * 64 + lane, row = (q * 43691) >> 19; vw[p] = row * (2688 - 192) + q * 16; mk[p] = __ballot(q - row * 12 != 11); }
#define ROT ((ct + 2 * rb) % 7)
        int cg = ROT;
        const int sx0 = (rb * 128 + 32 * tt) * 896, sw0 = (168 * ct) & ~15;
        unsigned ldn = lbase;
        auto issue1 = [&](int p) {
            if (p < 4) dma(rx, (p & 1) ? vx1 : vx0, sx0 + p * 7168 + cg * 128, ldn + (4 * tt + p) * 1024);
            else
                asm volatile("s_mov_b32 m0, %0\n\ts_mov_b64 exec, %4\n\ts_nop 2\n\tbuffer_load_dwordx4 %1, %2, %3 offen lds\n\ts_mov_b64 exec, -1"
                             :: "s"(ldn + XCH + (3 * tt + p - 4) * 1024), "v"(vw[p - 4]), "s"(rq), "s"(sw0 + cg * (64 * 2688)), "s"(mk[p - 4]) : "memory");
        };
    if (w >= 4) {
#pragma unroll 1
        for (int c = 0; c < 2; ++c) {
#pragma unroll
            for (int p = 0; p < 7; ++p) issue1(p);
            cg = cg == 6 ? 0 : cg + 1; ldn += CHB;
        }
    }
    ;
    SBAR();
    const rsrc_t rw = __builtin_amdgcn_make_buffer_rsrc(ws, 0, WS_END, 0x00020000);
    const f32x16 zero = {};
    const unsigned ONES = 0x3f803f80u;
    u32x2 ep;
    u32x4 rte[2], rto[2]; int rdst[2], rts[2];
    if (w >= 4) {
        __builtin_amdgcn_s_setprio(3);
        const int r16 = lane & 15, g16 = lane >> 4;
        const LAS char* xp = lds + (32 * tt + r16) * 128;
        const LAS char* wp = lds + XCH + (32 * (tt >> 1) + 8 * g16 + (r16 >> 2)) * 192 + ((168 * ct) & 15) + ((tt & 1) * 16 + (lane & 3) * 4) * 2;
        const u32x4 ones4 = {ONES, ONES, ONES, ONES};
        f32x4 sq[2] = {{}, {}}, cs[3] = {{}, {}, {}};
        u32x4 xf[4], wf[3];
        int sl = 0;
        auto rd = [&](int p) {
            if (p < 4) xf[p] = lds128(xp + sl + (p & 1) * 2048 + ((((p >> 1) * 4 + g16) ^ (r16 >> 1)) << 4));
            else { const u32x2 t0 = ldstr(wp + sl + (p - 4) * 56), t1 = ldstr(wp + sl + (p - 4) * 56 + 768); wf[p - 4] = (u32x4){t0[0], t0[1], t1[0], t1[1]}; }
            SBAR();
        };
        auto acc_ = [&](int p) {
            if (p < 4) sq[p & 1] = mfma16(xf[p], xf[p], sq[p & 1]);
            else cs[p - 4] = mfma16(ones4, wf[p - 4], cs[p - 4]);
            SBAR();
        };
        waitv<7>(); BAR();
        STAMP(1);
#pragma unroll
        for (int p = 0; p < 7; ++p) { issue1(p); rd(p); }
        cg = cg == 6 ? 0 : cg + 1; ldn += CHB;
#pragma unroll
        for (int p = 0; p < 7; ++p) issue1(p);
        cg = cg == 6 ? 0 : cg + 1; ldn += CHB;
#pragma unroll 1
        for (int c = 4; c < 7; ++c) {
            waitv<14>(); BAR();
            sl += CHB;
#pragma unroll
            for (int p = 0; p < 7; ++p) { issue1(p); acc_(p); rd(p); }
            cg = cg == 6 ? 0 : cg + 1; ldn = ldn == lbase + 4 * CHB ? lbase : ldn + CHB;
        }
        unsigned rv[2][8];
        ;
#pragma unroll
        for (int n = 0; n < 3; ++n) {
            rsrc_t rr; int rst, rs0, v0, v1;
            if (n < 2) {
                const u32x4 pa = lds128(lds + L_RT + (tt * 2 + n) * 32), pb = lds128(lds + L_RT + (tt * 2 + n) * 32 + 16);
                const u64 src = (u64)(unsigned)__builtin_amdgcn_readfirstlane(pa[0]) | ((u64)(unsigned)__builtin_amdgcn_readfirstlane(pa[1]) << 32);
                rr = __builtin_amdgcn_make_buffer_rsrc((void*)src, 0, __builtin_amdgcn_readfirstlane(pa[2]), 0x00020000);
                rst = __builtin_amdgcn_readfirstlane(pa[3]); rs0 = __builtin_amdgcn_readfirstlane(pb[0]);
                rdst[n] = __builtin_amdgcn_readfirstlane(pb[1]); rts[n] = __builtin_amdgcn_readfirstlane(pb[2]);
                v0 = 4 * h * rst + 4 * i; v1 = v0 | (h ? pb[3] : 0);
            }
            if (n == 0) waitv<14>(); else if (n == 1) waitv<15>(); else waitv<16>();
            if (n == 2) ;
            BAR();
            if (n == 2) ;
            sl = sl == 4 * CHB ? 0 : sl + CHB;
#pragma unroll
            for (int p = 0; p < 8; ++p) {
                if (p < 7) { acc_(p); rd(p); }
                if (n < 2) { rv[n][p] = __builtin_amdgcn_raw_buffer_load_b32(rr, p < 4 ? v0 : v1, rs0 + rst * (8 * (p >> 2) + (p & 3)), 0); SBAR(); }
            }
        }
        {
            const int c_ = jj >= 28 ? 6 : jj >> 2;
            if ((PRET & 1) && tt == 0) dma(__builtin_amdgcn_make_buffer_rsrc((void*)b1, 0, (c_ + 1) * 512, 0x00020000), l16, c_ * 512, lbase + L_B1);
            if ((PRET & 1) && tt == 1) dma(__builtin_amdgcn_make_buffer_rsrc((void*)b2, 0, (c_ + 1) * 128, 0x00020000), l16, c_ * 128, lbase + L_B2);
            if ((PRET & 4) && tt == 3) asm volatile("s_mov_b32 m0, %0\n\ts_nop 3\n\tbuffer_load_dword %1, %2, 0 offen lds" :: "s"(lbase + L_DUMP), "v"(lane * TSTRIDE + b * 128), "s"(rw) : "memory");
            if ((PRET & 2) && tt == 2) dma(__builtin_amdgcn_make_buffer_rsrc((void*)out, 0, 2048 * 896, 0x00020000), l16, (4 * xcd + (jj & 3)) * 64 * 896, lbase + L_DUMP);
        }
#pragma unroll
        for (int p = 0; p < 7; ++p) acc_(p);
        LAS float* csb = (LAS float*)(lds + L_CS);
        if (lane < 16) {
#pragma unroll
            for (int s = 0; s < 3; ++s) csb[(s * 2 + (tt >> 1)) * 32 + (tt & 1) * 16 + lane] = cs[s][0];
        }
#pragma unroll
        for (int tb = 0; tb < 2; ++tb) {
            const float q01 = (lane & 1) ? sq[tb][1] : sq[tb][0], q23 = (lane & 1) ? sq[tb][3] : sq[tb][2];
            if (g16 == (r16 >> 2)) ((LAS float*)(lds + L_XE))[tt * 32 + tb * 16 + r16] = (lane & 2) ? q23 : q01;
        }
        STAMP(2);
        BARRIER();
        __builtin_amdgcn_s_setprio(0);
        ep = __builtin_bit_cast(u32x2, *(const LAS u64*)(lds + L_CNT + 32) + 1);
#pragma unroll
        for (int n = 0; n < 2; ++n) {
#pragma unroll
            for (int j = 0; j < 4; ++j) { rte[n][j] = __builtin_amdgcn_perm(rv[n][2 * j + 1], rv[n][2 * j], 0x05040100u); rto[n][j] = __builtin_amdgcn_perm(rv[n][2 * j + 1], rv[n][2 * j], 0x07060302u); }
            if (n == 0 || tt < 3) { st_c(rte[n], rw, l16, rdst[n]); st_c(rto[n], rw, l16, rdst[n] + rts[n]); }
        }
        STAMP(3);
        waitv<0>();
        STAMP(4);
        arrive(lbase + L_CNT + 12, 1, 4, rw, WS_FR + b * 8, ep, lane);
    } else {
        auto early = [&]() {
        if (w == 0) {
            u64 pc; int len;
            asm volatile("s_getpc_b64 %0\n87:\n\ts_mov_b32 %1, 88f-87b" : "=s"(pc), "=s"(len));
            const rsrc_t rc = __builtin_amdgcn_make_buffer_rsrc((void*)(pc & ~127ull), 0, len, 0x00020000);
            dma(rc, l16, jj * 1024, lbase + L_DUMP);
        }
        if (w == 3) {
            asm volatile("s_mov_b32 m0, %0\n\ts_mov_b64 exec, 3\n\ts_nop 2\n\tbuffer_load_dword %1, %2, 0 offen sc1 lds\n\ts_mov_b64 exec, -1" :: "s"(lbase + L_CNT + 32), "v"(lane * 4 + WS_EPOCH), "s"(rw) : "memory");
        }
        };
        if (w == 0 && lane < 16 && (lane >> 1) != 4) ((LAS unsigned*)(lds + L_CNT))[lane] = 0u;
        if (!LATE0) early();
        if (lane < 2) {
            const int u = 7 * b + (lane && tt < 3 ? tt + 4 : tt);
            const bool is1 = u < 784, is2 = u < 1568;
            const int q = is1 ? u : (is2 ? u - 784 : u - 1568), nks = is1 ? 28 : (is2 ? 112 : 32);
            const int win = is1 ? q / 28 : (is2 ? q / 112 : q >> 5), ks = q - win * nks;
            const int r0 = is2 ? 64 * (ks >> 2) + ((ks >> 1) & 1) + 32 * (ks & 1) : 28 * (ks >> 1) + 16 * (ks & 1);
            const u64 src = (u64)(is1 ? w1 : (is2 ? w2 : wo));
            LAS u32x4* pr = (LAS u32x4*)(lds + L_RT + (tt * 2 + lane) * 32);
            pr[0] = (u32x4){(unsigned)src, (unsigned)(src >> 32), (unsigned)(is2 ? 2 * D_ * F_ : 2 * D_ * D_), (unsigned)(is1 ? 4 * F_ : (is2 ? 4 * D_ : 2 * D_))};
            pr[1] = (u32x4){(unsigned)(r0 * (is1 ? 2 * F_ : 2 * D_) + win * 128), (unsigned)((is1 ? WS_W1 : (is2 ? WS_W2 : WS_WO)) + (2 * win * nks + (!is1 && is2 ? ((ks >> 3) & 1) * 56 + (ks >> 4) * 8 + (ks & 7) : ks)) * 1024),
                            (unsigned)(nks * 1024), (!is2 && (ks & 1)) ? 0x40000000u : 0u};
        }
        const LAS char* xp[4];
#pragma unroll
        for (int ks = 0; ks < 4; ++ks) xp[ks] = lds + (32 * tt + i) * 128 + (((2 * ks + h) ^ ((i >> 1) & 7)) << 4);
        const int wcol = ((168 * ct) & 15) + (((lane >> 4) & 1) * 16 + (lane & 3) * 4) * 2;
        const LAS char* wp = lds + XCH + (8 * h + ((lane & 15) >> 2)) * 192 + wcol;
        const LAS char* wp2 = wp + ((lane & 19) == 19 ? 176 - wcol : 112);
        *(LAS u32x2*)(lds + tt * CHB + XCH + lane * 192 + 176) = (u32x2){ONES, ONES};
        if (w == 0) *(LAS u32x2*)(lds + 4 * CHB + XCH + lane * 192 + 176) = (u32x2){ONES, ONES};
        unsigned scl[3], pd2[3], pd3[3]; int dst[3];
#pragma unroll
        for (int s = 0; s < 3; ++s) {
            const int sec = 3 * ct + s, tl = (sec & 15) * 64 + rb * 4 + tt;
            scl[s] = sec < 16 ? __builtin_bit_cast(unsigned, 0.18898223650461361f * 1.4426950408889634f) : 0x3f800000u;
            pd2[s] = sec < 16 ? 0u : (sec < 32 ? ONES : 0x3f80u); pd3[s] = (sec >= 16 && sec < 32) ? 0x3f80u : 0u;
            dst[s] = sec < 16 ? WS_Q + tl * 2048 : WS_KV + tl * 4096 + (sec >= 32 ? 2048 : 0);
            asm volatile("" : "+s"(scl[s]), "+s"(pd2[s]), "+s"(pd3[s]), "+s"(dst[s]));
        }
        LAS char* vtw = lds + L_VT + w * 6912 + i * 72 + h * 8;
        const LAS char* vtr = lds + L_VT + w * 6912 + (8 * h + ((lane & 15) >> 2)) * 72 + ((lane >> 4) & 1) * 32 + (lane & 3) * 8;
        int f1o = WS_F1 + ((3 * ct + lane) * 2 + (rb >> 3)) * 128 + (rb & 7) * 8;
        PIN(vtw); PIN(vtr); PIN(f1o);
        f32x16 acc[3] = {zero, zero, zero};
        PIN(acc[0]); PIN(acc[1]); PIN(acc[2]);
        u32x4 xf[4], wf[4][3];
        auto rdw = [&](int ks, int s, const LAS char* p) {
            const u32x2 t0 = ldstr(p + ks * 3072 + (s & 1) * 56), t1 = ldstr(p + ks * 3072 + (s & 1) * 56 + 768);
            wf[ks][s] = (u32x4){t0[0], t0[1], t1[0], t1[1]};
        };
        BARRIER();
        STAMP(1);
#pragma unroll
        for (int ks = 0; ks < 4; ++ks) { xf[ks] = lds128(xp[ks]); rdw(ks, 0, wp); rdw(ks, 1, wp); rdw(ks, 2, wp2); SBAR(); }
        if (LATE0) early();
        int sl = CHB;
#pragma unroll 1
        for (int c = 1; c < 7; ++c) {
            if (c == 6) waitv<0>();
            BAR();
            const LAS char* p = wp + sl; const LAS char* p2 = wp2 + sl;
#pragma unroll
            for (int ks = 0; ks < 4; ++ks)
#pragma unroll
                for (int s = 0; s < 3; ++s) {
                    acc[s] = mfma(wf[ks][s], xf[ks], acc[s]); SBAR();
                    if (s == 2) xf[ks] = lds128(xp[ks] + sl);
                    rdw(ks, s, s == 2 ? p2 : p); SBAR();
                }
            sl = sl == 4 * CHB ? 0 : sl + CHB;
        }
#pragma unroll
        for (int ks = 0; ks < 4; ++ks) { acc[2] = mfma(wf[ks][2], xf[ks], acc[2]); SBAR(); }
        BAR();
        ep = __builtin_bit_cast(u32x2, *(const LAS u64*)(lds + L_CNT + 32) + 1);
        const float sqv = ((const LAS float*)(lds + L_XE))[tt * 32 + i];
        float cv[6];
#pragma unroll
        for (int s = 0; s < 6; ++s) cv[s] = ((const LAS float*)(lds + L_CS))[s * 32 + i];
        SBAR();
        acc[0] = mfma(wf[0][0], xf[0], acc[0]); SBAR();
        float lo_, sxv; both32(acc[2][12], lo_, sxv);
        const float mean = sxv * (1.f / D_);
        unsigned mh, ml; split2(-mean, mh, ml);
        u32x4 xe = {h ? 0u : (mh | (mh << 16)), h ? 0u : (ml | (ml << 16)), 0u, 0u};
        PIN(xe); SBAR();
        auto corr = [&](int s) {
            unsigned ch, cl; split2(cv[2 * s] + cv[2 * s + 1], ch, cl);
            const unsigned cw = h ? 0u : (ch | (cl << 16));
            acc[s] = mfma((u32x4){cw, cw, 0u, 0u}, xe, acc[s]); SBAR();
        };
        acc[0] = mfma(wf[1][0], xf[1], acc[0]); SBAR();
        corr(2);
        float rstd = __builtin_amdgcn_rsqf(fmaxf(sqv * (1.f / D_) - mean * mean, 0.f) + EPS);
        PIN(rstd); SBAR();
        u32x4 p0, p1;
        auto fin = [&](int s, int half) {
            const float sc = rstd * __uint_as_float(scl[s]);
            u32x4& p = half ? p1 : p0;
#pragma unroll
            for (int e = 0; e < 4; ++e) p[e] = pk2(acc[s][8 * half + 2 * e] * sc, acc[s][8 * half + 2 * e + 1] * sc);
            if (half && h) { p[2] = pd2[s]; p[3] = pd3[s]; }
            PIN(p); SBAR();
        };
        auto put = [&](int s) {
            if (3 * ct + s >= 32) {
                LAS u32x2* wq = (LAS u32x2*)vtw;
                wq[0] = (u32x2){p0[0], p0[1]}; wq[2] = (u32x2){p0[2], p0[3]}; wq[4] = (u32x2){p1[0], p1[1]}; wq[6] = (u32x2){p1[2], p1[3]};
                const u32x2 t0 = ldstr(vtr), t1 = ldstr(vtr + 288), t2 = ldstr(vtr + 1152), t3 = ldstr(vtr + 1440);
                xf[0] = (u32x4){t0[0], t0[1], t1[0], t1[1]}; wf[0][0] = (u32x4){t2[0], t2[1], t3[0], t3[1]};
            } else { xf[0] = p0; wf[0][0] = p1; }
            SBAR();
        };
        auto sto = [&](int s) { st_c(xf[0], rw, l16, dst[s]); st_c(wf[0][0], rw, l16 + 1024, dst[s]); SBAR(); };
        acc[0] = mfma(wf[2][0], xf[2], acc[0]); SBAR();
        acc[1] = mfma(wf[0][1], xf[0], acc[1]); SBAR();
        fin(2, 0);
        acc[0] = mfma(wf[3][0], xf[3], acc[0]); SBAR();
        corr(0);
        fin(2, 1);
        acc[1] = mfma(wf[1][1], xf[1], acc[1]); SBAR();
        put(2);
        acc[1] = mfma(wf[2][1], xf[2], acc[1]); SBAR();
        fin(0, 0);
        acc[1] = mfma(wf[3][1], xf[3], acc[1]); SBAR();
        corr(1);
        STAMP(2);
        fin(0, 1);
        sto(2); put(0);
        fin(1, 0); fin(1, 1);
        sto(0); put(1); sto(1);
        STAMP(3);
        waitv<0>();
        STAMP(4);
        arrive(lbase + L_CNT + 16, 1, 4, rw, f1o, ep, lane, 3);
    }
    ep[0] = __builtin_amdgcn_readfirstlane(ep[0]); ep[1] = __builtin_amdgcn_readfirstlane(ep[1]);

    const int head = 2 * xcd + (jj & 1), qb = jj >> 1, kh = w >> 2;
    const int r3 = 4 * xcd + (jj & 3), c3 = jj >= 28 ? 6 : (jj >> 2);
    u32x4 a1[28];
    const int w1s = WS_W1 + (8 * c3 + w) * 28 * 1024;
    const int kw = (w + ROTK * c3) & 7;
    f32x16 o;
    {
        const int kvs = WS_KV + head * 64 * 4096 + w * 4096;
        const unsigned ldst = lbase + w * 4096;
        const int wos = WS_WO + c3 * 65536 + w * 4096;
        const int kb = 2 * (lane & 7) + kh;
        const int fro = lane < 16 ? WS_F1 + (((16 << (lane >> 3)) + head) * 2 + (kb >> 3)) * 128 + (kb & 7) * 8
                      : WS_FR + (lane < 32 ? 112 + 16 * c3 + (lane - 16) : (lane < 48 ? 16 * c3 + 4 * (w >> 1) + (lane & 3) : ((1568 + 32 * c3 + 4 * w + (lane & 1) * 3) * 9363) >> 16)) * 8;
        const int fpo = WS_FP + (lane < 16 ? 8 * (2 * (r3 >> 1) + (lane & 1)) + (lane >> 1) : 8 * (4 * (lane < 23 ? lane - 16 : 0) + (r3 & 3)) + xcd) * 8;
        const u32x4 POISON = {~0u, ~0u, ~0u, ~0u};
        const LAS char* rd = lds + kh * 16384 + l16;
        const LAS char* rd2 = rd + 65536; asm volatile("" : "+v"(rd2));
        u32x4 q0, q1;
        const int odst = WS_O + ((qb * 4 + tt) * 32 + head * 2 + kh) * 1024;
        int po = odst, ph = jj < 28 ? WS_HID + (r3 * 224 + kh * 56 + 8 * c3 + 2 * tt) * 1024 : OOB, pg = jj < 28 ? WS_H + ((r3 * 2 + kh) * 28 + 4 * c3 + tt) * 1024 : OOB;
        int ps = (jj < 28 && w < 2) ? WS_ST + ((r3 * 2 + w) * 14 + 2 * c3) * 256 : OOB;
        bool done = false;
        volatile LAS unsigned* ring = (volatile LAS unsigned*)(lds + L_CNT) + 12;
        unsigned tgt = 0, one = 1; PIN(ring); PIN(one);
#pragma unroll 1
        for (int pass = 0; pass < 2; ++pass) {
            if (pass == 0) {
        if (w == POLLW) wait_flags(rw, WS_F1 + (lane < 16 ? ((16 << (lane >> 3)) + head) * 256 + (lane & 3) * 8 : (head * 2 + (qb >> 3)) * 128 + (qb & 7) * 8), ep);
        BAR();
        STAMP(5);
            }
            q0 = ld_c_async(rw, l16, WS_Q + (head * 64 + qb * 4 + tt) * 2048); q1 = ld_c_async(rw, l16, WS_Q + (head * 64 + qb * 4 + tt) * 2048 + 1024);
            dma_c4(rw, l16, kvs, ldst);
            dma_c4(rw, l16, kvs + 32768, ldst + 32768);
            u32x2 frv = ld_flag_async(rw, fro), fpv;
            waitv<5>(); PIN(q0); PIN(q1); BAR();
            STAMP(6);
            float m;
            if (pass == 0) {
                f32x16 s = mfma(lds128(lds + l16), q0, zero); s = mfma(lds128(lds + 1024 + l16), q1, s);
                m = s[0];
#pragma unroll
                for (int r = 1; r < 16; ++r) m = fmaxf(m, s[r]);
            } else {
                m = -INFINITY;
#pragma unroll 1
                for (int t = 0; t < 64; ++t) {
                    f32x16 s = mfma(ld_c(rw, l16, WS_KV + (head * 64 + t) * 4096), q0, zero); s = mfma(ld_c(rw, l16 + 1024, WS_KV + (head * 64 + t) * 4096), q1, s);
#pragma unroll
                    for (int r = 0; r < 16; ++r) m = fmaxf(m, s[r]);
                }
            }
            {
                const float n = -max32(m); const unsigned t0 = pk2(n, 0.f) & 0xffffu; const float r1 = n - bflo(t0);
                const unsigned t1 = pk2(r1, 0.f) & 0xffffu; const unsigned t2 = pk2(r1 - bflo(t1), 0.f) & 0xffffu;
                if (h) { q1[2] = t0 | (t1 << 16); q1[3] = t2; }
            }
            o = zero;
            f32x16 sA, sB; u32x4 k0, k1, v0, v1;
            sA = mfma(lds128(rd), q0, zero); sA = mfma(lds128(rd + 1024), q1, sA);
            k0 = lds128(rd + 4096); k1 = lds128(rd + 4096 + 1024);
            v0 = lds128(rd + 2048); v1 = lds128(rd + 3072);
#define EX(P, n) P[n] = __builtin_amdgcn_exp2f(P[n])
#define E8(P, n) do { EX(P, n); EX(P, n + 1); EX(P, n + 2); EX(P, n + 3); EX(P, n + 4); EX(P, n + 5); EX(P, n + 6); EX(P, n + 7); PIN(P); SBAR(); } while (0)
#define PRIO(n) do { __builtin_amdgcn_s_setprio(n); SBAR(); } while (0)
#define TPTR(t) (((((t) >> 2) & 2) ? rd2 : rd) + (((t) >> 2) & 1) * 32768 + ((t) & 3) * 4096)
#define ISD(t) ((t) < 2 || (((t) & 2) && (t) < 30))
#define OTH(t) ((t) >= 30 ? 14 + (t) - 30 : ((t) >> 2) * 2 - 2 + ((t) & 1))
#define PN(t) ((t) == 4 ? 4 : (t) == 5 ? 5 : (t) == 8 ? 6 : (t) == 12 ? 0 : (t) == 16 ? 1 : (t) == 20 ? 2 : (t) == 24 ? 3 : -1)
#define WN(t, s) ((s) && PN(t) >= 0 ? -1 : 2 * OTH(t) + (s) - ((t) > 4) - ((t) > 5) - ((t) > 8) - ((t) > 12) - ((t) > 16) - ((t) > 20) - ((t) > 24))
#define SLOT(t, s) do { \
            if (ISD(t)) { constexpr int st_ = (t) < 2 ? 2 : ((t) >> 2) + 3; \
                dma_co<(((t) & 1) * 2 + (s)) * 1024>(rw, l16, st_ < 8 ? kvs + st_ * 32768 : wos + (st_ - 8) * 32768, ldst + (st_ & 3) * 32768); } \
            else if (WN(t, s) < 0) { constexpr int pn_ = PN(t); \
                if (pn_ < 4) st_c(POISON, rw, l16 + (pn_ & 1) * 1024, ph + ((pn_ >> 1) & 1) * 112 * 1024); \
                else st_c(POISON, rw, pn_ == 6 && h ? OOB : l16, pn_ == 4 ? po : (pn_ == 5 ? pg : ps)); } \
            else if (WN(t, s) < NW1) { constexpr int wn_ = ISD(t) || WN(t, s) < 0 || WN(t, s) >= NW1 ? 0 : WN(t, s); a1[wn_] = ld_c(rw, l16 + (wn_ & 3) * 1024, w1s + (wn_ >> 2) * 4096); } \
            SBAR(); } while (0)
#define STEP(SC, SN, t) do { \
            constexpr bool arr_ = ((t) & 3) == 1 && (t) < 29, chk_ = ((t) & 3) == 2 && (t) < 30; \
            unsigned cv_; \
            if (USEBAR && chk_ && kh) { waitv<(t) == 2 ? 4 : ((t) == 6 ? 8 : 12)>(); BAR(); } \
            PRIO(0); E8(SC, 0); PRIO(1); if (chk_ && !USEBAR) { cv_ = *ring; SBAR(); } E8(SC, 8); PRIO(2); \
            if (USEBAR && chk_ && !kh) { waitv<(t) == 2 ? 4 : ((t) == 6 ? 8 : 12)>(); BAR(); } \
            if (arr_ && !USEBAR) { waitv<(t) == 1 ? 2 : ((t) == 5 ? 6 : 10)>(); asm volatile("s_mov_b64 exec, 1\n\tds_add_u32 %0, %1\n\ts_mov_b64 exec, -1" :: "v"(ring), "v"(one) : "memory"); SBAR(); } \
            if (chk_ && !USEBAR) { tgt += 8; if (__builtin_amdgcn_readfirstlane(cv_) < tgt) spin_lds(ring, tgt); asm volatile("" ::: "memory"); SBAR(); } \
            if ((t) == 0) { asm volatile("s_waitcnt vmcnt(0)" : "+v"(frv) :: "memory"); if (!flag_ok(frv, ep)) wait_flags(rw, fro, ep); SBAR(); }     \
            if ((t) == 18) { if (w == 0 && lane == 0) st_flag(rw, WS_FP + b * 8, ep); SBAR(); } \
            if ((t) < 31) { SN = mfma(k0, q0, zero); SBAR(); } \
            SLOT(t, 0); \
            if ((t) < 31) { SN = mfma(k1, q1, SN); SBAR(); } \
            SLOT(t, 1); \
            if ((t) == 21 && WARM2) { asm volatile("s_mov_b32 m0, %0\n\ts_nop 3\n\tbuffer_load_dword %1, %2, %3 offen sc1 lds" :: "s"(lbase + L_DUMP), "v"(lane < 49 ? lane * 128 : OOB), "s"(rw), "s"(WS_W2 + (jj * 8 + w) * 49 * 128) : "memory"); SBAR(); } \
            if ((t) == 30) { fpv = ld_flag_async(rw, fpo); SBAR(); } \
            o = pv(v0, SC, 0, o); SBAR(); \
            if ((t) < 30) { k0 = lds128(TPTR((t) + 2)); k1 = lds128(TPTR((t) + 2) + 1024); SBAR(); } \
            if ((t) < 31) { v0 = lds128(TPTR((t) + 1) + 2048); SBAR(); } \
            o = pv(v1, SC, 1, o); SBAR(); if ((t) < 31) { v1 = lds128(TPTR((t) + 1) + 3072); SBAR(); } \
            } while (0)
#define STEP4(t) STEP(sA, sB, t); STEP(sB, sA, (t) + 1); STEP(sA, sB, (t) + 2); STEP(sB, sA, (t) + 3)
            CSTAMP(22); STAMP(23);
            STEP4(0); STEP4(4); STEP4(8); STEP4(12); STEP4(16); STEP4(20); STEP4(24); STEP4(28);
            __builtin_amdgcn_s_setprio(0);
            STAMP(7); CSTAMP(24);
            LAS f32x4* red = (LAS f32x4*)(lds + L_RED) + lane;
            LAS float* lsum = (LAS float*)(lds + L_FLAG + 64);
            f32x8 mine;
            if (kh) {
                red[w * 128] = __builtin_shufflevector(o, o, 0, 1, 2, 3); red[w * 128 + 64] = __builtin_shufflevector(o, o, 4, 5, 6, 7);
                mine = __builtin_shufflevector(o, o, 8, 9, 10, 11, 12, 13, 14, 15);
            } else {
                red[w * 128] = __builtin_shufflevector(o, o, 8, 9, 10, 11); red[w * 128 + 64] = __builtin_shufflevector(o, o, 12, 13, 14, 15);
                mine = __builtin_shufflevector(o, o, 0, 1, 2, 3, 4, 5, 6, 7);
            }
            lsum[w * 64 + lane] = o[12];
            BARRIER();
            const float l = lsum[tt * 64 + 32 + i] + lsum[(tt + 4) * 64 + 32 + i];
            const f32x4 t0 = red[(w ^ 4) * 128], t1 = red[(w ^ 4) * 128 + 64];
            const float rl = __builtin_amdgcn_rcpf(l);
            mine += __builtin_shufflevector(t0, t1, 0, 1, 2, 3, 4, 5, 6, 7);
            float amax = l;
#pragma unroll
            for (int r = 0; r < 8; ++r) amax = fmaxf(amax, fabsf(mine[r]));
            const bool bad = pass == 0 && __any(!(amax <= 1e30f && l == l));
            st_c((u32x4){pk2(mine[0] * rl, mine[1] * rl), pk2(mine[2] * rl, mine[3] * rl), pk2(mine[4] * rl, mine[5] * rl), pk2(mine[6] * rl, mine[7] * rl)},
                 rw, l16, (bad || done) ? OOB : odst);
#pragma unroll
            for (int k = 0; k < NW1; ++k) PIN(a1[k]);
            done = !bad;
            if (bad) ((volatile LAS unsigned*)(lds + L_CNT))[6] = 1u;
            waitv<1>(); PIN(fpv);
            if (!flag_ok(fpv, ep)) wait_flags(rw, fpo, ep);
            BARRIER();
            if (__builtin_amdgcn_readfirstlane(((volatile LAS unsigned*)(lds + L_CNT))[6]) == 0u) break;
            waitv<0>();
            po = ph = pg = ps = OOB;
        }
    }
    STAMP(8);
    if (jj >= 28) {
        if (b == 255 && w == 0) {
#pragma unroll 1
            for (int n = 0; n < 4; ++n) wait_flags(rw, WS_FR + (n * 64 + lane) * 8, ep);
            if (lane == 0) st_flag(rw, WS_EPOCH, ep);
        }
        STAMP_OUT();
        return;
    }

    float cs1 = 0.f;
    unsigned bias, b2v;
    u32x4 a2[28];
    const int w2s = WS_W2 + (2 * c3 * 112 + 7 * kw) * 1024;
#define W2O(q) ((((q) & 1) * 112 + ((q) >= 14 ? 56 - 7 : 0) + ((q) >> 1)) * 1024)
    {
        const int nt2 = w & 1, rt = (w >> 1) & 1;
        f32x16 acc;
        u32x4 xr[4];
        auto ldw = [&](int k) {
            if (k < 4) a1[24 + k] = ld_c(rw, l16 + k * 1024, w1s + 6 * 4096);
            else a2[k - 4] = ld_c(rw, l16, w2s + W2O(k - 4));
        };
        auto fetch = [&]() {
            dma_c4(rw, l16, WS_O + r3 * 65536 + w * 8192, lbase + 65536 + w * 8192);
            dma_c4(rw, l16, WS_O + r3 * 65536 + w * 8192 + 4096, lbase + 65536 + w * 8192 + 4096);
        };
        float cs2 = 0.f;
        STAMP(9);
        fetch();
        {
            const rsrc_t rx2 = __builtin_amdgcn_make_buffer_rsrc((void*)x, 0, w >= 4 ? 2048 * 896 : 0, 0x00020000);
#pragma unroll
            for (int g = 0; g < 4; ++g) xr[g] = __builtin_amdgcn_raw_buffer_load_b128(rx2, i * 896 + 16 * h + 32 * g, (r3 * 64 + rt * 32) * 896 + c3 * 128, 0);
        }
        if (PRET & 1) bias = *(const LAS u16*)(lds + L_B1 + ((w >> 1) * 64 + 2 * i + (w & 1)) * 2);
        else {
        bias = b1[(4 * c3 + (w >> 1)) * 64 + 2 * i + (w & 1)];
        b2v = *(const unsigned*)(b2 + c3 * 64 + 2 * i);
        }
        asm volatile("" ::: "memory"); SBAR();
#pragma unroll
        for (int k = 0; k < NE2; ++k) ldw(k);
        SBAR();
#pragma unroll
        for (int k = 0; k < 24; ++k) { cs1 = dot2(a1[k][0], ONES, cs1); cs2 = dot2(a1[k][1], ONES, cs2); cs1 = dot2(a1[k][2], ONES, cs1); cs2 = dot2(a1[k][3], ONES, cs2); }
        PIN(cs1); PIN(cs2);
        waitv<NE2>();
#pragma unroll
        for (int g = 0; g < 4; ++g)
#pragma unroll
            for (int e = 0; e < 4; ++e) acc[4 * g + e] = nt2 ? bfhi(xr[g][e]) : bflo(xr[g][e]);
        PIN(acc); PIN(bias);
        if (!(PRET & 1) && w == 6) ((LAS unsigned*)(lds + L_B2))[i] = b2v;
#pragma unroll 1
        for (int tries = 0; tries < 4096; ++tries) {
            unsigned m = 0;
#pragma unroll
            for (int p = 0; p < 8; ++p) m = pmax(m, lds128(lds + 65536 + w * 8192 + p * 1024 + l16));
            if (!poisoned(m)) break;
            fetch();
            asm volatile("s_waitcnt vmcnt(0)" ::: "memory");
        }
        BAR();
        STAMP(10);
        if (w >= 4) {
            const LAS char* pa = lds + nt2 * 32768 + l16; const LAS char* pb = lds + 65536 + rt * 32768 + l16;
            u32x4 fa[4], fb[4];
#pragma unroll
            for (int k = 0; k < 4; ++k) { fa[k] = lds128(pa + k * 1024); fb[k] = lds128(pb + k * 1024); }
            SBAR();
#pragma unroll
            for (int k = 0; k < 32; ++k) {
                acc = mfma(fa[k & 3], fb[k & 3], acc); SBAR();
                if (k < 28) { fa[k & 3] = lds128(pa + (k + 4) * 1024); fb[k & 3] = lds128(pb + (k + 4) * 1024); SBAR(); }
                if (k % 4 == 0 && k / 4 < NP3A) { ldw(NE2 + k / 4); SBAR(); }
            }
            BAR();
            const int rtile = r3 * 2 + rt, nt = 2 * c3 + nt2;
            const u32x4 p0 = pack8(acc, 0), p1 = pack8(acc, 1);
            st_c(p0, rw, l16, WS_H + (rtile * 28 + 2 * nt) * 1024); st_c(p1, rw, l16 + 1024, WS_H + (rtile * 28 + 2 * nt) * 1024);
            float s = 0.f, q = 0.f, s2 = 0.f, q2 = 0.f;
#pragma unroll
            for (int e = 0; e < 4; ++e) { s = dot2(p0[e], ONES, s); q = dot2(p0[e], p0[e], q); s2 = dot2(p1[e], ONES, s2); q2 = dot2(p1[e], p1[e], q2); }
            s = sum32(s + s2); q = sum32(q + q2);
            __builtin_amdgcn_raw_buffer_store_b32(__float_as_uint(h ? q : s), rw, lane * 4, WS_ST + (rtile * 14 + nt) * 256, 16);
            *(LAS u32x4*)(lds + L_HS + tt * 2048 + l16) = p0; *(LAS u32x4*)(lds + L_HS + tt * 2048 + 1024 + l16) = p1;
            STAMP(11);
        } else {
#pragma unroll
            for (int k = NE2; k < 22; ++k) ldw(k);
            BAR(); __builtin_amdgcn_s_sleep(E3SLEEP); }
#pragma unroll
        for (int k = 24; k < 28; ++k) { cs1 = dot2(a1[k][0], ONES, cs1); cs2 = dot2(a1[k][1], ONES, cs2); cs1 = dot2(a1[k][2], ONES, cs1); cs2 = dot2(a1[k][3], ONES, cs2); }
        cs1 = sum32(cs1 + cs2);
    }
    u32x4 bf[14];
    int ha[4], hb[4];
#pragma unroll
    for (int q = 0; q < 4; ++q) { ha[q] = WS_HID + (r3 * 224 + 7 * kw + (q >> 1) * 56 + (q & 1) * 4) * 1024; hb[q] = ha[q] + 112 * 1024; }
#define LDH(k, so) asm volatile("buffer_load_dwordx4 %0, %1, %2, %3 offen offset:%4 sc1" : "=v"(bf[k]) : "v"(l16), "s"(rw), "s"(so[((k) / 7) * 2 + (((k) % 7) >> 2)]), "n"((((k) % 7) & 3) * 1024) : "memory");
#define FOR7(M, g, so) M(7 * g, so) M(7 * g + 1, so) M(7 * g + 2, so) M(7 * g + 3, so) M(7 * g + 4, so) M(7 * g + 5, so) M(7 * g + 6, so)
    {
        const int ft = 8 * c3 + w;
        unsigned ch, cl; split2(cs1, ch, cl);
        u32x4 ae = {h ? 0u : (ch | (cl << 16)), h ? 0u : (ch | (cl << 16)), h ? 0u : (bias | (bias << 16)), 0u};
        PIN(ae);
        STAMP(12);
#pragma unroll 1
        for (int tries = 0; tries < 4096; ++tries) {
            dma_c4(rw, l16, WS_H + r3 * 57344 + w * 7168, lbase + w * 7168);
#pragma unroll
            for (int p = 4; p < 7; ++p) dma_c(rw, l16, WS_H + r3 * 57344 + w * 7168 + p * 1024, lbase + w * 7168 + p * 1024);
            dma_c(rw, l16, w < 7 ? WS_ST + r3 * 7168 + w * 1024 : OOB, lbase + 57344 + w * 1024);
            waitv<0>();
            unsigned m = pmax(0u, lds128(lds + 57344 + w * 1024 + l16));
#pragma unroll
            for (int p = 0; p < 7; ++p) m = pmax(m, lds128(lds + w * 7168 + p * 1024 + l16));
                        if (!poisoned(m)) break;
        }
        BARRIER();
        auto park = [&](int q) { dma_c(rw, l16, w2s + W2O(q), lbase + 65536 + w * 10240 + (q - 18) * 1024); };
        if (w >= 4) {
#pragma unroll
            for (int q = NE2 - 4 + NP3A; q < 18; ++q) a2[q] = ld_c(rw, l16, w2s + W2O(q));
#pragma unroll
            for (int q = 18; q < 28; ++q) park(q);
        }
        STAMP(13); CSTAMP(29);
        u32x4 fb[4];
#pragma unroll
        for (int k = 0; k < 4; ++k) fb[k] = lds128(lds + k * 1024 + l16);
#pragma unroll
        for (int rt = 0; rt < 2; ++rt) {
            f32x16 acc = zero;
            const LAS float* sp = (const LAS float*)(lds + 57344) + rt * 14 * 64 + lane;
            float st = 0.f, sv[14];
            u32x4 be; float g1, g2, sd, mean, var, rs;
            if (rt) __builtin_amdgcn_s_setprio(2); else __builtin_amdgcn_s_setprio(3);
            SBAR();
#pragma unroll
            for (int k = 0; k < 28; ++k) {
                acc = mfma(a1[k], fb[k & 3], acc); SBAR();
                if (rt * 28 + k + 4 < 56) fb[k & 3] = lds128(lds + (rt * 28 + k + 4) * 1024 + l16);
                if (k < 7) { sv[2 * k] = sp[2 * k * 64]; sv[2 * k + 1] = sp[(2 * k + 1) * 64]; }
                if (k >= 4 && k < 11) { st += sv[2 * (k - 4)] + sv[2 * (k - 4) + 1]; PIN(st); }
                if (k == 12) { both32(st, mean, var); mean *= 1.f / D_; PIN(mean); PIN(var); }
                if (k == 13) { var = fmaxf(var * (1.f / D_) - mean * mean, 0.f); rs = __builtin_amdgcn_rsqf(var + EPS); PIN(var); PIN(rs); }
                if (k == 14) { var = __builtin_amdgcn_rsqf(var * rs * rs + EPS); PIN(var); }
                if (k == 15) { rs *= var; sd = __builtin_amdgcn_rcpf(rs); g1 = -2.3022081981f * rs; PIN(rs); PIN(sd); PIN(g1); }
                if (k == 16) { g2 = -0.1029432397f * rs * rs * rs; be[0] = pk2(-mean, -mean); PIN(g2); PIN(be[0]); }
                if (k == 17) { const float r_ = -mean - bflo(be[0]); be[1] = pk2(r_, r_); be[0] = h ? 0u : be[0]; PIN(be[0]); PIN(be[1]); }
                if (k == 18) { be[2] = pk2(sd, sd); be[1] = h ? 0u : be[1]; PIN(be[1]); PIN(be[2]); }
                if (k == 19) { be[2] = pk2(sd, sd - bflo(be[2])); be[2] = h ? 0u : be[2]; be[3] = 0u; PIN(be); }
                if (k == 21) acc = mfma(ae, be, acc);
                if (rt == 1 && k == 13) waitv<2>();
                if (rt == 1 && k >= 14 && k < 24) a2[k + 4] = lds128(lds + 65536 + w * 10240 + (k - 14) * 1024 + l16);
                SBAR();
            }
            if (rt) __builtin_amdgcn_s_setprio(0); else __builtin_amdgcn_s_setprio(1);
            if (rt) {
#pragma unroll
                for (int q = 0; q < 28; ++q) PIN(a2[q]);
                asm volatile("s_nop 4" : "+s"(ha[0]), "+s"(ha[1]), "+s"(ha[2]), "+s"(ha[3]), "+s"(hb[0]), "+s"(hb[1]), "+s"(hb[2]), "+s"(hb[3]));
                STAMP(16);
            }
            SBAR();
#pragma unroll
            for (int hf = 0; hf < 2; ++hf) {
                const f32x8 a = hf ? __builtin_shufflevector(acc, acc, 8, 9, 10, 11, 12, 13, 14, 15) : __builtin_shufflevector(acc, acc, 0, 1, 2, 3, 4, 5, 6, 7);
                f32x8 u;
#define ST8(EXPR) _Pragma("unroll") for (int j = 0; j < 8; ++j) { float t_ = (EXPR); asm volatile("" : "+v"(t_)); u[j] = t_; } SBAR();
#define LA(n) if (rt) { if (hf) { LDH(7 + n, ha) } else { LDH(n, ha) } SBAR(); } else if (n < 5 && w < 4) { park(18 + 5 * hf + n); SBAR(); }
                LA(0) ST8(a[j] * a[j]) LA(1) ST8(__builtin_fmaf(u[j], g2, g1)) LA(2) ST8(u[j] * a[j]) LA(3) ST8(__builtin_amdgcn_exp2f(u[j]))
                LA(4) ST8(__builtin_fmaf(u[j], sd, sd)) LA(5) ST8(__builtin_amdgcn_rcpf(u[j])) LA(6) ST8(u[j] * a[j])
#pragma unroll
                for (int j = 0; j < 8; ++j) acc[8 * hf + j] = u[j];
            }
            const int dst = WS_HID + ((r3 * 2 + rt) * 112 + kh * 56 + 8 * c3 + 2 * tt) * 1024;
            u32x4 g0 = pack8(acc, 0), g8 = pack8(acc, 1);
            st_c(g0, rw, l16, dst); st_c(g8, rw, l16 + 1024, dst);
            SBAR();
            if (rt == 0) STAMP(14);
        }
        STAMP(15); CSTAMP(30);
    }
    {
        volatile LAS unsigned* cnt = (volatile LAS unsigned*)(lds + L_CNT);
        auto put = [&](const f32x16& acc0, const f32x16& acc1, int rt) {
#pragma unroll
            for (int p = 0; p < 8; ++p) {
                LAS f32x2* red = (LAS f32x2*)(lds + (rt ? 0 : 65536) + w * 8192 + p * 1024 + ((l16 + (p & 3) * 64) & 1023));
                red[0] = (f32x2){acc0[2 * p], acc0[2 * p + 1]}; red[1] = (f32x2){acc1[2 * p], acc1[2 * p + 1]};
            }
            SBAR();
        };
        const rsrc_t ro = __builtin_amdgcn_make_buffer_rsrc(out, 0, 2048 * 896, 0x00020000);
        auto fin = [&](int rt, int rg) {
            const int row = 4 * rg + (lane >> 4), cq = lane & 15, hh = (cq >> 1) & 1, p = (cq & 1) + 2 * (cq >> 2), ln = hh * 32 + row;
            const LAS f32x4* red = (const LAS f32x4*)(lds + (rt ? 0 : 65536) + p * 1024 + ((ln * 16 + (p & 3) * 64) & 1023));
            const u32x2 bb = *(const LAS u32x2*)(lds + L_B2 + cq * 8);
            const LAS char* hp = lds + L_HS + rt * 4096 + (p >> 2) * 1024 + ln * 16 + (p & 3) * 4;
            const unsigned h0 = *(const LAS unsigned*)hp, h1 = *(const LAS unsigned*)(hp + 2048);
            const f32x4 r = ((red[0] + red[512]) + (red[1024] + red[1536])) + ((red[2048] + red[2560]) + (red[3072] + red[3584]));
            const u32x2 res = {pk2(r[0] + bflo(bb[0]) + bflo(h0), r[2] + bfhi(bb[0]) + bflo(h1)), pk2(r[1] + bflo(bb[1]) + bfhi(h0), r[3] + bfhi(bb[1]) + bfhi(h1))};
            __builtin_amdgcn_raw_buffer_store_b64(res, ro, row * 896 + cq * 8, (r3 * 64 + rt * 32) * 896 + c3 * 128, OUTAUX);
        };
        asm volatile("s_mov_b64 exec, 1\n\tds_add_u32 %0, %1\n\ts_mov_b64 exec, -1" :: "v"(lbase + L_CNT + 40), "v"(1u) : "memory");
#define WAIT7(g, n) asm volatile("s_waitcnt vmcnt(" #n ")" : "+v"(bf[7 * g]), "+v"(bf[7 * g + 1]), "+v"(bf[7 * g + 2]), "+v"(bf[7 * g + 3]), "+v"(bf[7 * g + 4]), "+v"(bf[7 * g + 5]), "+v"(bf[7 * g + 6]) :: "memory")
#define CHECK7(g, n, so, id) do { WAIT7(g, n); _Pragma("unroll 1") for (int tries = 0; tries < 4096; ++tries) { unsigned m = 0; \
            _Pragma("unroll") for (int k = 7 * g; k < 7 * g + 7; ++k) m = pmax(m, bf[k]); \
            if (!poisoned(m)) break; \
            TRY(id); FOR7(LDH, g, so) WAIT7(g, 0); } } while (0)
#define MM(k, so) acc0 = mfma(a2[2 * (k)], bf[k], acc0); acc1 = mfma(a2[2 * (k) + 1], bf[k], acc1); SBAR();
#define MML(k, so) MM(k, so) LDH((k) - 7, so) SBAR();
        f32x16 acc0 = zero, acc1 = zero;
        CHECK7(0, 9, ha, 25);
        STAMP(17);
        FOR7(MM, 0, hb)
        CHECK7(1, 2, ha, 26);
        FOR7(MML, 1, hb)
        STAMP(18);
        spin_lds(cnt + 10, 8);
        if (w < 4) { put(acc0, acc1, 0); __builtin_amdgcn_s_sleep(B1SLEEP); }
        FOR7(LDH, 1, hb)
        if (w >= 4) put(acc0, acc1, 0);
        asm volatile("s_waitcnt lgkmcnt(0)\n\ts_mov_b64 exec, 1\n\tds_add_u32 %0, %1\n\ts_mov_b64 exec, -1" :: "v"(lbase + L_CNT + 44), "v"(1u) : "memory");
        acc0 = zero; acc1 = zero;
        CHECK7(0, 7, hb, 27);
        STAMP(19);
        FOR7(MM, 0, hb)
        CHECK7(1, 0, hb, 28);
        FOR7(MM, 1, hb)
        STAMP(20);
        put(acc0, acc1, 1);
        if (w < 4) {
#pragma unroll 1
            for (int spin = 0; spin < (1 << 24) && cnt[11] != 8u; ++spin) __builtin_amdgcn_s_sleep(1);
            asm volatile("" ::: "memory");
            fin(0, w); fin(0, w + 4);
        }
        BARRIER();
        fin(1, w);
    }
    STAMP(21);
    STAMP_OUT();
    asm volatile("88:");
}

extern "C" void kernel_launch(void* const* d_in, const int* in_sizes, int n_in, void* d_out, int out_size, void* d_ws, size_t ws_size, hipStream_t stream) {
    (void)hipFuncSetAttribute((const void*)fused_block, hipFuncAttributeMaxDynamicSharedMemorySize, LDS_BYTES);
    fused_block<<<256, 512, LDS_BYTES, stream>>>((const u16*)d_in[0], (const u16*)d_in[1], (const u16*)d_in[2], (const u16*)d_in[4], (const u16*)d_in[5],
                                                 (const u16*)d_in[6], (const u16*)d_in[7], (u16*)d_out, (char*)d_ws);
}
```

```cpp
#include <hip/hip_runtime.h>
#include <stdint.h>

typedef unsigned short u16;
typedef unsigned long long u64;
typedef __bf16 bf16x2 __attribute__((ext_vector_type(2)));
typedef __bf16 bf16x8 __attribute__((ext_vector_type(8)));
typedef __bf16 bf16x16 __attribute__((ext_vector_type(16)));
typedef float f32x2 __attribute__((ext_vector_type(2)));
typedef float f32x4 __attribute__((ext_vector_type(4)));
typedef float f32x8 __attribute__((ext_vector_type(8)));
typedef float f32x16 __attribute__((ext_vector_type(16)));
typedef unsigned u32x2 __attribute__((ext_vector_type(2)));
typedef unsigned u32x4 __attribute__((ext_vector_type(4)));
typedef short s16x4 __attribute__((ext_vector_type(4)));
typedef __amdgpu_buffer_rsrc_t rsrc_t;
#define LAS __attribute__((address_space(3)))
#define DI __device__ __forceinline__
#define SBAR() __builtin_amdgcn_sched_barrier(0)
#define PIN(x) asm volatile("" : "+v"(x))

constexpr int D_ = 448, F_ = 1792;
constexpr float EPS = 1e-5f;
constexpr int NW1 = 24;
constexpr int TRIES = 1 << 16;
constexpr int WS_EPOCH = 0;
constexpr int NREP = 2;
constexpr int WS_F1 = 1024;
constexpr int WS_FR = WS_F1 + 48 * NREP * 512;
constexpr int WS_FP = WS_FR + 2048;
constexpr int WS_ST = 131072;
constexpr int WS_Q = WS_ST + 64 * 14 * 256;
constexpr int WS_KV = WS_Q + 16 * 64 * 2048;
constexpr int WS_O = WS_KV + 16 * 64 * 4096;
constexpr int WS_H = WS_O + 64 * 32 * 1024;
constexpr int WS_W1 = WS_H + 64 * 28 * 1024;
constexpr int WS_W2 = WS_W1 + 56 * 28 * 1024;
constexpr int WS_WO = WS_W2 + 14 * 112 * 1024;
constexpr int WS_HID = WS_WO + 14 * 32 * 1024;
constexpr int WS_END = WS_HID + 64 * 112 * 1024;
constexpr int OOB = 0x40000000;

constexpr int XCH = 16384, WCH = 12288, CHB = XCH + WCH;
constexpr int L_MISC = 5 * CHB;
constexpr int L_CS = L_MISC, L_XE = L_MISC + 768, L_RT = L_XE + 512;
constexpr int L_VT = 2 * CHB;
constexpr int L_RED = 131072;
constexpr int L_LS = L_RED + 16384;
constexpr int L_HS = L_LS + 2048;
constexpr int L_B2 = L_HS + 8192, L_B1 = L_B2 + 1024;
constexpr int L_CNT = 163840 - 1024 - 64;
constexpr int L_DUMP = 163840 - 1024;
constexpr int LDS_BYTES = 163840;

DI f32x16 mfma(u32x4 a, u32x4 b, f32x16 c) {
    return __builtin_amdgcn_mfma_f32_32x32x16_bf16(__builtin_bit_cast(bf16x8, a), __builtin_bit_cast(bf16x8, b), c, 0, 0, 0);
}
DI f32x4 mfma16(u32x4 a, u32x4 b, f32x4 c) {
    return __builtin_amdgcn_mfma_f32_16x16x32_bf16(__builtin_bit_cast(bf16x8, a), __builtin_bit_cast(bf16x8, b), c, 0, 0, 0);
}
DI f32x16 pv(u32x4 v, const f32x16& p, int s, f32x16 o) {
    f32x8 ph = {p[8 * s], p[8 * s + 1], p[8 * s + 2], p[8 * s + 3], p[8 * s + 4], p[8 * s + 5], p[8 * s + 6], p[8 * s + 7]};
    return __builtin_amdgcn_smfmac_f32_32x32x32_bf16(__builtin_bit_cast(bf16x8, v), __builtin_bit_cast(bf16x16, ph), o, 0xDDDD, 0, 0);
}
DI unsigned pk2(float lo, float hi) { f32x2 v = {lo, hi}; return __builtin_bit_cast(unsigned, __builtin_convertvector(v, bf16x2)); }
DI float bflo(unsigned u) { return __uint_as_float(u << 16); }
DI float bfhi(unsigned u) { return __uint_as_float(u & 0xffff0000u); }
DI float dot2(unsigned a, unsigned b, float c) { return __builtin_amdgcn_fdot2_f32_bf16(__builtin_bit_cast(bf16x2, a), __builtin_bit_cast(bf16x2, b), c, false); }
DI void both32(float v, float& lo, float& hi) {
    auto r = __builtin_amdgcn_permlane32_swap(__float_as_uint(v), __float_as_uint(v), false, false);
    lo = __uint_as_float(r[0]); hi = __uint_as_float(r[1]);
}
DI float sum32(float v) { float a, b; both32(v, a, b); return a + b; }
DI float max32(float v) { float a, b; both32(v, a, b); return fmaxf(a, b); }
DI u32x4 lds128(const LAS char* p) { return *(const LAS u32x4*)p; }
DI u32x2 ldstr(const LAS char* p) { return __builtin_bit_cast(u32x2, __builtin_amdgcn_ds_read_tr16_b64_v4i16((LAS s16x4*)p)); }
DI void split2(float v, unsigned& hi, unsigned& lo) { hi = pk2(v, 0.f) & 0xffffu; lo = pk2(v - bflo(hi), 0.f) & 0xffffu; }
DI u32x4 pack8(const f32x16& a, int s) {
    u32x4 r;
    r[0] = pk2(a[8 * s + 0], a[8 * s + 1]); r[1] = pk2(a[8 * s + 2], a[8 * s + 3]);
    r[2] = pk2(a[8 * s + 4], a[8 * s + 5]); r[3] = pk2(a[8 * s + 6], a[8 * s + 7]);
    return r;
}
DI void dma(rsrc_t rs, int voff, int soff, unsigned lds_addr) {
    asm volatile("s_mov_b32 m0, %0\n\ts_nop 3\n\tbuffer_load_dwordx4 %1, %2, %3 offen lds" :: "s"(lds_addr), "v"(voff), "s"(rs), "s"(soff) : "memory");
}
DI void dma_c(rsrc_t rs, int voff, int soff, unsigned lds_addr) {
    asm volatile("s_mov_b32 m0, %0\n\ts_nop 3\n\tbuffer_load_dwordx4 %1, %2, %3 offen sc1 lds" :: "s"(lds_addr), "v"(voff), "s"(rs), "s"(soff) : "memory");
}
DI void dma_c4(rsrc_t rs, int voff, int soff, unsigned lds_addr) {
    asm volatile("s_mov_b32 m0, %0\n\ts_nop 3\n\tbuffer_load_dwordx4 %1, %2, %3 offen sc1 lds\n\tbuffer_load_dwordx4 %1, %2, %3 offen offset:1024 sc1 lds\n\t"
                 "buffer_load_dwordx4 %1, %2, %3 offen offset:2048 sc1 lds\n\tbuffer_load_dwordx4 %1, %2, %3 offen offset:3072 sc1 lds"
                 :: "s"(lds_addr), "v"(voff), "s"(rs), "s"(soff) : "memory");
}
DI u32x4 ld_c(rsrc_t rs, int voff, int soff) { return __builtin_amdgcn_raw_buffer_load_b128(rs, voff, soff, 16); }
DI void st_c(u32x4 v, rsrc_t rs, int voff, int soff) {
    __builtin_amdgcn_raw_buffer_store_b128(v, rs, voff, soff, 16);
    asm volatile("s_nop 1" :: "v"(v) : "memory");
}
DI u32x2 ld_flag(rsrc_t rs, int voff) { asm volatile("" ::: "memory"); return __builtin_amdgcn_raw_buffer_load_b64(rs, voff, 0, 16); }
DI void st_flag(rsrc_t rs, int voff, u32x2 e) { __builtin_amdgcn_raw_buffer_store_b64(e, rs, voff, 0, 16); }
template <int N> DI void waitv() {
    asm volatile("s_waitcnt vmcnt(%0)" :: "n"(N) : "memory");
    if (N == 0) __builtin_amdgcn_s_waitcnt(0x0F70);
}
#define BAR() asm volatile("s_barrier" ::: "memory")
#define BARRIER() asm volatile("s_waitcnt lgkmcnt(0)\n\ts_barrier" ::: "memory")
DI bool flag_ok(u32x2 f, u32x2 e) { return __all(f[0] == e[0] && f[1] == e[1]); }
template <int SLEEP = 1> DI void wait_flags(rsrc_t rs, int voff, u32x2 e) {
#pragma unroll 1
    for (int spin = 0; spin < (1 << 18); ++spin) {
        if (flag_ok(ld_flag(rs, voff), e)) break;
        __builtin_amdgcn_s_sleep(SLEEP);
    }
    asm volatile("" ::: "memory");
}
DI void arrive(unsigned cnt_addr, unsigned n, rsrc_t rs, int foff, u32x2 e, int lane, int nl = 1) {
    unsigned old;
    asm volatile("s_mov_b64 exec, 1\n\tds_add_rtn_u32 %0, %1, %2\n\ts_mov_b64 exec, -1\n\ts_waitcnt lgkmcnt(0)" : "=&v"(old) : "v"(cnt_addr), "v"(1u) : "memory");
    if (__builtin_amdgcn_readfirstlane(old) + 1u == n && lane < nl) st_flag(rs, foff, e);
}
template <int OFF> DI void dma_co(rsrc_t rs, int voff, int soff, unsigned lds_addr) {
    asm volatile("s_mov_b32 m0, %0\n\ts_nop 3\n\tbuffer_load_dwordx4 %1, %2, %3 offen offset:%4 sc1 lds" :: "s"(lds_addr), "v"(voff), "s"(rs), "s"(soff), "n"(OFF) : "memory");
}
DI u32x4 ld_c_async(rsrc_t rs, int voff, int soff) { u32x4 r; asm volatile("s_nop 4\n\tbuffer_load_dwordx4 %0, %1, %2, %3 offen sc1" : "=v"(r) : "v"(voff), "s"(rs), "s"(soff) : "memory"); return r; }
DI u32x2 ld_flag_async(rsrc_t rs, int voff) { u32x2 r; asm volatile("buffer_load_dwordx2 %0, %1, %2, 0 offen sc1" : "=v"(r) : "v"(voff), "s"(rs) : "memory"); return r; }
DI unsigned pmax(unsigned m, u32x4 v) { return max(max(max(max(v[0], v[1]), m), v[2]), v[3]); }
DI bool poisoned(unsigned m) { return __any(m == 0xffffffffu); }
DI void spin_lds(volatile LAS unsigned* p, unsigned tgt) {
#pragma unroll 1
    for (int spin = 0; spin < (1 << 24); ++spin) { if (__builtin_amdgcn_readfirstlane(*p) >= tgt) break; __builtin_amdgcn_s_sleep(1); }
    asm volatile("" ::: "memory");
}

__global__ __launch_bounds__(512, 2) void fused_block(const u16* __restrict__ x, const u16* __restrict__ wqkv, const u16* __restrict__ wo,
                                                       const u16* __restrict__ w1, const u16* __restrict__ b1, const u16* __restrict__ w2,
                                                       const u16* __restrict__ b2, u16* __restrict__ out, char* __restrict__ ws) {
    extern __shared__ __attribute__((aligned(16))) char smem[];
    LAS char* lds = (LAS char*)smem;
    const unsigned lbase = (unsigned)(uintptr_t)lds;
    const int lane = threadIdx.x & 63, w = __builtin_amdgcn_readfirstlane(threadIdx.x >> 6);
    const int i = lane & 31, h = lane >> 5, l16 = lane * 16;
    const int b = blockIdx.x, xcd = b & 7, jj = b >> 3;
    const int rb = 4 * (xcd >> 1) + (jj & 3), ct = 8 * (xcd & 1) + (jj >> 2);
    const int tt = w & 3;
    const rsrc_t rx = __builtin_amdgcn_make_buffer_rsrc((void*)x, 0, 2048 * 896, 0x00020000);
    const rsrc_t rq = __builtin_amdgcn_make_buffer_rsrc((void*)wqkv, 0, 448 * 2688, 0x00020000);
    const int vx0 = (lane >> 3) * 896 + (((lane & 7) ^ (lane >> 4)) << 4), vx1 = vx0 ^ 64;
    int vw[3]; u64 mk[3];
#pragma unroll
    for (int p = 0; p < 3; ++p) { const int q = (3 * tt + p) * 64 + lane, row = (q * 43691) >> 19; vw[p] = row * (2688 - 192) + q * 16; mk[p] = __ballot(q - row * 12 != 11); }
    int cg = ((ct + rb) & 1) * 2 + (xcd & 3);
    const int sx0 = (rb * 128 + 32 * tt) * 896, sw0 = (168 * ct) & ~15;
    unsigned ldn = lbase;
    auto issue1 = [&](int p) {
        if (p < 4) dma(rx, (p & 1) ? vx1 : vx0, sx0 + p * 7168 + cg * 128, ldn + (4 * tt + p) * 1024);
        else
            asm volatile("s_mov_b32 m0, %0\n\ts_mov_b64 exec, %4\n\ts_nop 2\n\tbuffer_load_dwordx4 %1, %2, %3 offen lds\n\ts_mov_b64 exec, -1"
                         :: "s"(ldn + XCH + (3 * tt + p - 4) * 1024), "v"(vw[p - 4]), "s"(rq), "s"(sw0 + cg * (64 * 2688)), "s"(mk[p - 4]) : "memory");
    };
    if (w >= 4) {
#pragma unroll 1
        for (int c = 0; c < 2; ++c) {
#pragma unroll
            for (int p = 0; p < 7; ++p) issue1(p);
            cg = cg == 6 ? 0 : cg + 1; ldn += CHB;
        }
    }
    SBAR();
    const rsrc_t rw = __builtin_amdgcn_make_buffer_rsrc(ws, 0, WS_END, 0x00020000);
    const f32x16 zero = {};
    const unsigned ONES = 0x3f803f80u;
    u32x2 ep;
    if (w >= 4) {
        __builtin_amdgcn_s_setprio(3);
        const int r16 = lane & 15, g16 = lane >> 4;
        const LAS char* xp = lds + (32 * tt + r16) * 128;
        const LAS char* wp = lds + XCH + (32 * (tt >> 1) + 8 * g16 + (r16 >> 2)) * 192 + ((168 * ct) & 15) + ((tt & 1) * 16 + (lane & 3) * 4) * 2;
        const u32x4 ones4 = {ONES, ONES, ONES, ONES};
        f32x4 sq[2] = {{}, {}}, cs[3] = {{}, {}, {}};
        u32x4 xf[4], wf[3];
        int sl = 0;
        auto rd = [&](int p) {
            if (p < 4) xf[p] = lds128(xp + sl + (p & 1) * 2048 + ((((p >> 1) * 4 + g16) ^ (r16 >> 1)) << 4));
            else { const u32x2 t0 = ldstr(wp + sl + (p - 4) * 56), t1 = ldstr(wp + sl + (p - 4) * 56 + 768); wf[p - 4] = (u32x4){t0[0], t0[1], t1[0], t1[1]}; }
            SBAR();
        };
        auto acc_ = [&](int p) {
            if (p < 4) sq[p & 1] = mfma16(xf[p], xf[p], sq[p & 1]);
            else cs[p - 4] = mfma16(ones4, wf[p - 4], cs[p - 4]);
            SBAR();
        };
        waitv<7>(); BAR();
#pragma unroll
        for (int p = 0; p < 7; ++p) { issue1(p); rd(p); }
        cg = cg == 6 ? 0 : cg + 1; ldn += CHB;
#pragma unroll
        for (int p = 0; p < 7; ++p) issue1(p);
        cg = cg == 6 ? 0 : cg + 1; ldn += CHB;
#pragma unroll 1
        for (int c = 4; c < 7; ++c) {
            waitv<14>(); BAR();
            sl += CHB;
#pragma unroll
            for (int p = 0; p < 7; ++p) { issue1(p); acc_(p); rd(p); }
            cg = cg == 6 ? 0 : cg + 1; ldn = ldn == lbase + 4 * CHB ? lbase : ldn + CHB;
        }
        unsigned rv[2][8]; int rdst[2], rts[2];
#pragma unroll
        for (int n = 0; n < 3; ++n) {
            rsrc_t rr; int rst, rs0, v0, v1;
            if (n < 2) {
                const u32x4 pa = lds128(lds + L_RT + (tt * 2 + n) * 32), pb = lds128(lds + L_RT + (tt * 2 + n) * 32 + 16);
                const u64 src = (u64)(unsigned)__builtin_amdgcn_readfirstlane(pa[0]) | ((u64)(unsigned)__builtin_amdgcn_readfirstlane(pa[1]) << 32);
                rr = __builtin_amdgcn_make_buffer_rsrc((void*)src, 0, __builtin_amdgcn_readfirstlane(pa[2]), 0x00020000);
                rst = __builtin_amdgcn_readfirstlane(pa[3]); rs0 = __builtin_amdgcn_readfirstlane(pb[0]);
                rdst[n] = __builtin_amdgcn_readfirstlane(pb[1]); rts[n] = __builtin_amdgcn_readfirstlane(pb[2]);
                v0 = 4 * h * rst + 4 * i; v1 = v0 | (h ? pb[3] : 0);
            }
            if (n == 0) waitv<14>(); else if (n == 1) waitv<15>(); else waitv<16>();
            BAR();
            sl = sl == 4 * CHB ? 0 : sl + CHB;
#pragma unroll
            for (int p = 0; p < 8; ++p) {
                if (p < 7) { acc_(p); rd(p); }
                if (n < 2) { rv[n][p] = __builtin_amdgcn_raw_buffer_load_b32(rr, p < 4 ? v0 : v1, rs0 + rst * (8 * (p >> 2) + (p & 3)), 0); SBAR(); }
            }
        }
#pragma unroll
        for (int p = 0; p < 7; ++p) acc_(p);
        LAS float* csb = (LAS float*)(lds + L_CS);
        if (lane < 16) {
#pragma unroll
            for (int s = 0; s < 3; ++s) csb[(s * 2 + (tt >> 1)) * 32 + (tt & 1) * 16 + lane] = cs[s][0];
        }
#pragma unroll
        for (int tb = 0; tb < 2; ++tb) {
            const float q01 = (lane & 1) ? sq[tb][1] : sq[tb][0], q23 = (lane & 1) ? sq[tb][3] : sq[tb][2];
            if (g16 == (r16 >> 2)) ((LAS float*)(lds + L_XE))[tt * 32 + tb * 16 + r16] = (lane & 2) ? q23 : q01;
        }
        BARRIER();
        __builtin_amdgcn_s_setprio(0);
        ep = __builtin_bit_cast(u32x2, *(const LAS u64*)(lds + L_CNT + 32) + 1);
#pragma unroll
        for (int n = 0; n < 2; ++n) {
            u32x4 ev, od;
#pragma unroll
            for (int j = 0; j < 4; ++j) { ev[j] = __builtin_amdgcn_perm(rv[n][2 * j + 1], rv[n][2 * j], 0x05040100u); od[j] = __builtin_amdgcn_perm(rv[n][2 * j + 1], rv[n][2 * j], 0x07060302u); }
            if (n == 0 || tt < 3) { st_c(ev, rw, l16, rdst[n]); st_c(od, rw, l16, rdst[n] + rts[n]); }
        }
        {
            const int c_ = jj >= 28 ? 6 : jj >> 2;
            if (tt == 0) dma(__builtin_amdgcn_make_buffer_rsrc((void*)b1, 0, (c_ + 1) * 512, 0x00020000), l16, c_ * 512, lbase + L_B1);
            if (tt == 1) dma(__builtin_amdgcn_make_buffer_rsrc((void*)b2, 0, (c_ + 1) * 128, 0x00020000), l16, c_ * 128, lbase + L_B2);
#pragma unroll
            for (int p = 0; p < 2; ++p) dma(rx, p ? vx1 : vx0, ((4 * xcd + (jj & 3)) * 64 + 16 * tt + 8 * p) * 896 + c_ * 128, lbase + L_HS + (2 * tt + p) * 1024);
            if (tt == 2) dma(__builtin_amdgcn_make_buffer_rsrc((void*)out, 0, 2048 * 896, 0x00020000), l16, (4 * xcd + (jj & 3)) * 64 * 896, lbase + L_DUMP);
        }
        waitv<0>();
        arrive(lbase + L_CNT + 12, 4, rw, WS_FR + b * 8, ep, lane);
    } else {
        if (w == 0) {
            u64 pc; int len;
            asm volatile("s_getpc_b64 %0\n87:\n\ts_mov_b32 %1, 88f-87b" : "=s"(pc), "=s"(len));
            const rsrc_t rc = __builtin_amdgcn_make_buffer_rsrc((void*)(pc & ~127ull), 0, len, 0x00020000);
            dma(rc, l16, jj * 1024, lbase + L_DUMP);
        }
        if (w == 3) {
            asm volatile("s_mov_b32 m0, %0\n\ts_mov_b64 exec, 3\n\ts_nop 2\n\tbuffer_load_dword %1, %2, 0 offen sc1 lds\n\ts_mov_b64 exec, -1" :: "s"(lbase + L_CNT + 32), "v"(lane * 4 + WS_EPOCH), "s"(rw) : "memory");
        }
        if (w == 0 && lane < 16 && (lane >> 1) != 4) ((LAS unsigned*)(lds + L_CNT))[lane] = 0u;
        if (lane < 2) {
            const int u = 7 * b + (lane && tt < 3 ? tt + 4 : tt);
            const bool is1 = u < 784, is2 = u < 1568;
            const int q = is1 ? u : (is2 ? u - 784 : u - 1568), nks = is1 ? 28 : (is2 ? 112 : 32);
            const int win = is1 ? q / 28 : (is2 ? q / 112 : q >> 5), ks = q - win * nks;
            const int r0 = is2 ? 64 * (ks >> 2) + ((ks >> 1) & 1) + 32 * (ks & 1) : 28 * (ks >> 1) + 16 * (ks & 1);
            const u64 src = (u64)(is1 ? w1 : (is2 ? w2 : wo));
            LAS u32x4* pr = (LAS u32x4*)(lds + L_RT + (tt * 2 + lane) * 32);
            pr[0] = (u32x4){(unsigned)src, (unsigned)(src >> 32), (unsigned)(is2 ? 2 * D_ * F_ : 2 * D_ * D_), (unsigned)(is1 ? 4 * F_ : (is2 ? 4 * D_ : 2 * D_))};
            pr[1] = (u32x4){(unsigned)(r0 * (is1 ? 2 * F_ : 2 * D_) + win * 128), (unsigned)((is1 ? WS_W1 : (is2 ? WS_W2 : WS_WO)) + (2 * win * nks + (!is1 && is2 ? ((ks >> 3) & 1) * 56 + (ks >> 4) * 8 + (ks & 7) : ks)) * 1024),
                            (unsigned)(nks * 1024), (!is2 && (ks & 1)) ? 0x40000000u : 0u};
        }
        const LAS char* xp[4];
#pragma unroll
        for (int ks = 0; ks < 4; ++ks) xp[ks] = lds + (32 * tt + i) * 128 + (((2 * ks + h) ^ ((i >> 1) & 7)) << 4);
        const int wcol = ((168 * ct) & 15) + (((lane >> 4) & 1) * 16 + (lane & 3) * 4) * 2;
        const LAS char* wp = lds + XCH + (8 * h + ((lane & 15) >> 2)) * 192 + wcol;
        const LAS char* wp2 = wp + ((lane & 19) == 19 ? 176 - wcol : 112);
        *(LAS u32x2*)(lds + tt * CHB + XCH + lane * 192 + 176) = (u32x2){ONES, ONES};
        if (w == 0) *(LAS u32x2*)(lds + 4 * CHB + XCH + lane * 192 + 176) = (u32x2){ONES, ONES};
        unsigned scl[3], pd2[3], pd3[3]; int dst[3];
#pragma unroll
        for (int s = 0; s < 3; ++s) {
            const int sec = 3 * ct + s, tl = (sec & 15) * 64 + rb * 4 + tt;
            scl[s] = sec < 16 ? __builtin_bit_cast(unsigned, 0.18898223650461361f * 1.4426950408889634f) : 0x3f800000u;
            pd2[s] = sec < 16 ? 0u : (sec < 32 ? ONES : 0x3f80u); pd3[s] = (sec >= 16 && sec < 32) ? 0x3f80u : 0u;
            dst[s] = sec < 16 ? WS_Q + tl * 2048 : WS_KV + tl * 4096 + (sec >= 32 ? 2048 : 0);
            asm volatile("" : "+s"(scl[s]), "+s"(pd2[s]), "+s"(pd3[s]), "+s"(dst[s]));
        }
        LAS char* vtw = lds + L_VT + w * 2304 + i * 72 + h * 8;
        const LAS char* vtr = lds + L_VT + w * 2304 + (8 * h + ((lane & 15) >> 2)) * 72 + ((lane >> 4) & 1) * 32 + (lane & 3) * 8;
        int f1o = WS_F1 + (3 * ct * NREP + lane) * 512 + (rb >> 2) * 128 + (rb & 3) * 8;
        PIN(vtw); PIN(vtr); PIN(f1o);
        f32x16 acc[3] = {zero, zero, zero};
        PIN(acc[0]); PIN(acc[1]); PIN(acc[2]);
        u32x4 xf[4], wf[4][3];
        auto rdw = [&](int ks, int s, const LAS char* p) {
            const u32x2 t0 = ldstr(p + ks * 3072 + (s & 1) * 56), t1 = ldstr(p + ks * 3072 + (s & 1) * 56 + 768);
            wf[ks][s] = (u32x4){t0[0], t0[1], t1[0], t1[1]};
        };
        BARRIER();
#pragma unroll
        for (int ks = 0; ks < 4; ++ks) { xf[ks] = lds128(xp[ks]); rdw(ks, 0, wp); rdw(ks, 1, wp); rdw(ks, 2, wp2); SBAR(); }
        int sl = CHB;
#pragma unroll 1
        for (int c = 1; c < 7; ++c) {
            if (c == 6) waitv<0>();
            BAR();
            const LAS char* p = wp + sl; const LAS char* p2 = wp2 + sl;
#pragma unroll
            for (int ks = 0; ks < 4; ++ks)
#pragma unroll
                for (int s = 0; s < 3; ++s) {
                    acc[s] = mfma(wf[ks][s], xf[ks], acc[s]); SBAR();
                    if (s == 2) xf[ks] = lds128(xp[ks] + sl);
                    rdw(ks, s, s == 2 ? p2 : p); SBAR();
                }
            sl = sl == 4 * CHB ? 0 : sl + CHB;
        }
#pragma unroll
        for (int ks = 0; ks < 4; ++ks) { acc[2] = mfma(wf[ks][2], xf[ks], acc[2]); SBAR(); }
        BAR();
        ep = __builtin_bit_cast(u32x2, *(const LAS u64*)(lds + L_CNT + 32) + 1);
        const float sqv = ((const LAS float*)(lds + L_XE))[tt * 32 + i];
        float cv[6];
#pragma unroll
        for (int s = 0; s < 6; ++s) cv[s] = ((const LAS float*)(lds + L_CS))[s * 32 + i];
        SBAR();
        acc[0] = mfma(wf[0][0], xf[0], acc[0]); SBAR();
        float lo_, sxv; both32(acc[2][12], lo_, sxv);
        const float mean = sxv * (1.f / D_);
        unsigned mh, ml; split2(-mean, mh, ml);
        u32x4 xe = {h ? 0u : (mh | (mh << 16)), h ? 0u : (ml | (ml << 16)), 0u, 0u};
        PIN(xe); SBAR();
        auto corr = [&](int s) {
            unsigned ch, cl; split2(cv[2 * s] + cv[2 * s + 1], ch, cl);
            const unsigned cw = h ? 0u : (ch | (cl << 16));
            acc[s] = mfma((u32x4){cw, cw, 0u, 0u}, xe, acc[s]); SBAR();
        };
        acc[0] = mfma(wf[1][0], xf[1], acc[0]); SBAR();
        corr(2);
        float rstd = __builtin_amdgcn_rsqf(fmaxf(sqv * (1.f / D_) - mean * mean, 0.f) + EPS);
        PIN(rstd); SBAR();
        u32x4 p0, p1;
        auto fin = [&](int s, int half) {
            const float sc = rstd * __uint_as_float(scl[s]);
            u32x4& p = half ? p1 : p0;
#pragma unroll
            for (int e = 0; e < 4; ++e) p[e] = pk2(acc[s][8 * half + 2 * e] * sc, acc[s][8 * half + 2 * e + 1] * sc);
            if (half && h) { p[2] = pd2[s]; p[3] = pd3[s]; }
            PIN(p); SBAR();
        };
        auto put = [&](int s) {
            if (3 * ct + s >= 32) {
                LAS u32x2* wq = (LAS u32x2*)vtw;
                wq[0] = (u32x2){p0[0], p0[1]}; wq[2] = (u32x2){p0[2], p0[3]}; wq[4] = (u32x2){p1[0], p1[1]}; wq[6] = (u32x2){p1[2], p1[3]};
                const u32x2 t0 = ldstr(vtr), t1 = ldstr(vtr + 288), t2 = ldstr(vtr + 1152), t3 = ldstr(vtr + 1440);
                xf[0] = (u32x4){t0[0], t0[1], t1[0], t1[1]}; wf[0][0] = (u32x4){t2[0], t2[1], t3[0], t3[1]};
            } else { xf[0] = p0; wf[0][0] = p1; }
            SBAR();
        };
        auto sto = [&](int s) { st_c(xf[0], rw, l16, dst[s]); st_c(wf[0][0], rw, l16 + 1024, dst[s]); SBAR(); };
        acc[0] = mfma(wf[2][0], xf[2], acc[0]); SBAR();
        acc[1] = mfma(wf[0][1], xf[0], acc[1]); SBAR();
        fin(2, 0);
        acc[0] = mfma(wf[3][0], xf[3], acc[0]); SBAR();
        corr(0);
        fin(2, 1);
        acc[1] = mfma(wf[1][1], xf[1], acc[1]); SBAR();
        put(2);
        acc[1] = mfma(wf[2][1], xf[2], acc[1]); SBAR();
        fin(0, 0);
        acc[1] = mfma(wf[3][1], xf[3], acc[1]); SBAR();
        corr(1);
        fin(0, 1);
        sto(2); put(0);
        fin(1, 0); fin(1, 1);
        sto(0); put(1); sto(1);
        waitv<0>();
        arrive(lbase + L_CNT + 16, 4, rw, f1o, ep, lane, 3 * NREP);
    }
    ep[0] = __builtin_amdgcn_readfirstlane(ep[0]); ep[1] = __builtin_amdgcn_readfirstlane(ep[1]);

    const int head = 2 * xcd + (jj & 1), qb = jj >> 1, kh = w >> 2;
    const int r3 = 4 * xcd + (jj & 3), c3 = jj >= 28 ? 6 : (jj >> 2);
    u32x4 a1[28];
    const int w1s = WS_W1 + (8 * c3 + w) * 28 * 1024;
    const int kw = (w + c3) & 7;
    auto fetch = [&]() {
        dma_c4(rw, l16, (jj < 28 ? WS_O : OOB) + r3 * 65536 + w * 8192, lbase + 65536 + w * 8192);
        dma_c4(rw, l16, (jj < 28 ? WS_O : OOB) + r3 * 65536 + w * 8192 + 4096, lbase + 65536 + w * 8192 + 4096);
    };
    f32x16 o;
    float cs1, cs2;
    {
        const int kvs = WS_KV + head * 64 * 4096 + w * 4096;
        const unsigned ldst = lbase + w * 4096;
        const int wos = WS_WO + c3 * 65536 + w * 4096;
        const int kb = 2 * (lane & 7) + kh;
        const int fko = WS_F1 + (((16 << ((lane >> 3) & 1)) + head) * NREP + qb % NREP) * 512 + (kb >> 2) * 128 + (kb & 3) * 8;
        const int fro = WS_FR + (lane < 32 ? 112 + 16 * c3 + (lane & 15) : (lane < 48 ? 16 * c3 + 4 * (w >> 1) + (lane & 3) : ((1568 + 32 * c3 + 4 * w + (lane & 1) * 3) * 9363) >> 16)) * 8;
        const int fpo = WS_FP + (lane < 16 ? 8 * (2 * (r3 >> 1) + (lane & 1)) + (lane >> 1) : 8 * (4 * (lane < 23 ? lane - 16 : 0) + (r3 & 3)) + xcd) * 8;
        const u32x4 POISON = {~0u, ~0u, ~0u, ~0u};
        const LAS char* rd = lds + kh * 16384 + l16;
        const LAS char* rd2 = rd + 65536; asm volatile("" : "+v"(rd2));
        u32x4 q0, q1;
        const int odst = WS_O + ((qb * 4 + tt) * 32 + head * 2 + kh) * 1024;
        int po = odst, ph = jj < 28 ? WS_HID + (r3 * 224 + kh * 56 + 8 * c3 + 2 * tt) * 1024 : OOB, pg = jj < 28 ? WS_H + ((r3 * 2 + kh) * 28 + 4 * c3 + tt) * 1024 : OOB;
        int ps = (jj < 28 && w < 2) ? WS_ST + ((r3 * 2 + w) * 14 + 2 * c3) * 256 : OOB;
        bool done = false;
        volatile LAS unsigned* ring = (volatile LAS unsigned*)(lds + L_CNT) + 12;
        unsigned tgt = 0, one = 1; PIN(ring); PIN(one);
#pragma unroll 1
        for (int pass = 0; pass < 2; ++pass) {
            if (pass == 0) {
                if (w == 7) wait_flags<3>(rw, WS_F1 + (qb % NREP) * 512 + (lane < 16 ? ((16 << (lane >> 3)) + head) * NREP * 512 + (lane & 3) * 8 : head * NREP * 512 + (qb >> 2) * 128 + (qb & 3) * 8), ep);
                BAR();
            }
            q0 = ld_c_async(rw, l16, WS_Q + (head * 64 + qb * 4 + tt) * 2048); q1 = ld_c_async(rw, l16, WS_Q + (head * 64 + qb * 4 + tt) * 2048 + 1024);
            dma_c4(rw, l16, kvs, ldst);
            dma_c4(rw, l16, kvs + 32768, ldst + 32768);
            u32x2 frv = ld_flag_async(rw, fko), fpv;
            waitv<5>(); PIN(q0); PIN(q1); BAR();
            float m;
            if (pass == 0) {
                f32x16 s = mfma(lds128(lds + l16), q0, zero); s = mfma(lds128(lds + 1024 + l16), q1, s);
                m = s[0];
#pragma unroll
                for (int r = 1; r < 16; ++r) m = fmaxf(m, s[r]);
            } else {
                m = -INFINITY;
#pragma unroll 1
                for (int t = 0; t < 64; ++t) {
                    f32x16 s = mfma(ld_c(rw, l16, WS_KV + (head * 64 + t) * 4096), q0, zero); s = mfma(ld_c(rw, l16 + 1024, WS_KV + (head * 64 + t) * 4096), q1, s);
#pragma unroll
                    for (int r = 0; r < 16; ++r) m = fmaxf(m, s[r]);
                }
            }
            {
                const float n = -max32(m); const unsigned t0 = pk2(n, 0.f) & 0xffffu; const float r1 = n - bflo(t0);
                const unsigned t1 = pk2(r1, 0.f) & 0xffffu; const unsigned t2 = pk2(r1 - bflo(t1), 0.f) & 0xffffu;
                if (h) { q1[2] = t0 | (t1 << 16); q1[3] = t2; }
            }
            o = zero;
            f32x16 sA, sB; u32x4 k0, k1, v0, v1;
            sA = mfma(lds128(rd), q0, zero); sA = mfma(lds128(rd + 1024), q1, sA);
            k0 = lds128(rd + 4096); k1 = lds128(rd + 4096 + 1024);
            v0 = lds128(rd + 2048); v1 = lds128(rd + 3072);
#define EX(P, n) P[n] = __builtin_amdgcn_exp2f(P[n])
#define E8(P, n) do { EX(P, n); EX(P, n + 1); EX(P, n + 2); EX(P, n + 3); EX(P, n + 4); EX(P, n + 5); EX(P, n + 6); EX(P, n + 7); PIN(P); SBAR(); } while (0)
#define PRIO(n) do { __builtin_amdgcn_s_setprio(n); SBAR(); } while (0)
#define TPTR(t) (((((t) >> 2) & 2) ? rd2 : rd) + (((t) >> 2) & 1) * 32768 + ((t) & 3) * 4096)
#define ISD(t) ((t) < 2 || (((t) & 2) && (t) < 30))
#define OTH(t) ((t) >= 30 ? 14 + (t) - 30 : ((t) >> 2) * 2 - 2 + ((t) & 1))
#define PN(t) ((t) == 4 ? 4 : (t) == 5 ? 5 : (t) == 8 ? 6 : (t) == 12 ? 0 : (t) == 16 ? 1 : (t) == 20 ? 2 : (t) == 24 ? 3 : -1)
#define WN(t, s) ((s) && PN(t) >= 0 ? -1 : 2 * OTH(t) + (s) - ((t) > 4) - ((t) > 5) - ((t) > 8) - ((t) > 12) - ((t) > 16) - ((t) > 20) - ((t) > 24))
#define SLOT(t, s) do { \
            if (ISD(t)) { constexpr int st_ = (t) < 2 ? 2 : ((t) >> 2) + 3; \
                dma_co<(((t) & 1) * 2 + (s)) * 1024>(rw, l16, st_ < 8 ? kvs + st_ * 32768 : wos + (st_ - 8) * 32768, ldst + (st_ & 3) * 32768); } \
            else if (WN(t, s) < 0) { constexpr int pn_ = PN(t); \
                if (pn_ < 4) st_c(POISON, rw, l16 + (pn_ & 1) * 1024, ph + ((pn_ >> 1) & 1) * 112 * 1024); \
                else st_c(POISON, rw, pn_ == 6 && h ? OOB : l16, pn_ == 4 ? po : (pn_ == 5 ? pg : ps)); } \
            else if (WN(t, s) < NW1) { constexpr int wn_ = ISD(t) || WN(t, s) < 0 || WN(t, s) >= NW1 ? 0 : WN(t, s); a1[wn_] = ld_c(rw, l16 + (wn_ & 3) * 1024, w1s + (wn_ >> 2) * 4096); } \
            SBAR(); } while (0)
#define STEP(SC, SN, t) do { \
            constexpr bool arr_ = ((t) & 3) == 1 && (t) < 29, chk_ = ((t) & 3) == 2 && (t) < 30; \
            unsigned cv_; \
            if ((t) == 31) SLOT(t, 0); \
            PRIO(0); E8(SC, 0); PRIO(1); if (chk_) { cv_ = *ring; SBAR(); } if ((t) == 31) { o = pv(v0, SC, 0, o); SBAR(); } E8(SC, 8); PRIO(2); \
            if (arr_) { waitv<(t) == 1 ? 2 : ((t) == 5 ? 6 : 10)>(); asm volatile("s_mov_b64 exec, 1\n\tds_add_u32 %0, %1\n\ts_mov_b64 exec, -1" :: "v"(ring), "v"(one) : "memory"); SBAR(); } \
            if (chk_) { tgt += 8; if (__builtin_amdgcn_readfirstlane(cv_) < tgt) spin_lds(ring, tgt); asm volatile("" ::: "memory"); SBAR(); } \
            if ((t) == 0) { asm volatile("s_waitcnt vmcnt(0)" : "+v"(frv) :: "memory"); if (!flag_ok(frv, ep)) wait_flags<12>(rw, fko, ep); SBAR(); }     \
            if ((t) == 2) { frv = ld_flag_async(rw, fro); SBAR(); } \
            if ((t) == 4) { asm volatile("s_waitcnt vmcnt(4)" : "+v"(frv) :: "memory"); if (!flag_ok(frv, ep)) wait_flags<12>(rw, fro, ep); SBAR(); }     \
            if ((t) == 18) { if (w == 0 && lane == 0) st_flag(rw, WS_FP + b * 8, ep); SBAR(); } \
            if ((t) < 31) { SN = mfma(k0, q0, zero); SBAR(); } \
            if ((t) < 31) SLOT(t, 0); \
            if ((t) < 31) { SN = mfma(k1, q1, SN); SBAR(); } \
            SLOT(t, 1); \
            if ((t) == 27) { fpv = ld_flag_async(rw, fpo); SBAR(); } \
            if ((t) < 31) { o = pv(v0, SC, 0, o); SBAR(); } \
            if ((t) < 30) { k0 = lds128(TPTR((t) + 2)); k1 = lds128(TPTR((t) + 2) + 1024); SBAR(); } \
            if ((t) < 31) { v0 = lds128(TPTR((t) + 1) + 2048); SBAR(); } \
            o = pv(v1, SC, 1, o); SBAR(); if ((t) < 31) { v1 = lds128(TPTR((t) + 1) + 3072); SBAR(); } \
            } while (0)
#define STEP4(t) STEP(sA, sB, t); STEP(sB, sA, (t) + 1); STEP(sA, sB, (t) + 2); STEP(sB, sA, (t) + 3)
            STEP4(0); STEP4(4); STEP4(8); STEP4(12); STEP4(16); STEP4(20); STEP4(24); STEP4(28);
            LAS f32x4* red = (LAS f32x4*)(lds + L_RED) + lane;
            LAS float* lsum = (LAS float*)(lds + L_LS);
            f32x8 mine;
            if (kh) {
                red[w * 128] = __builtin_shufflevector(o, o, 0, 1, 2, 3); red[w * 128 + 64] = __builtin_shufflevector(o, o, 4, 5, 6, 7);
                mine = __builtin_shufflevector(o, o, 8, 9, 10, 11, 12, 13, 14, 15);
            } else {
                red[w * 128] = __builtin_shufflevector(o, o, 8, 9, 10, 11); red[w * 128 + 64] = __builtin_shufflevector(o, o, 12, 13, 14, 15);
                mine = __builtin_shufflevector(o, o, 0, 1, 2, 3, 4, 5, 6, 7);
            }
            lsum[w * 64 + lane] = o[12];
            BARRIER();
            const float l = lsum[tt * 64 + 32 + i] + lsum[(tt + 4) * 64 + 32 + i];
            const f32x4 t0 = red[(w ^ 4) * 128], t1 = red[(w ^ 4) * 128 + 64];
            const float rl = __builtin_amdgcn_rcpf(l);
            mine += __builtin_shufflevector(t0, t1, 0, 1, 2, 3, 4, 5, 6, 7);
            const float asum = ((l + fabsf(mine[0])) + (fabsf(mine[1]) + fabsf(mine[2]))) + ((fabsf(mine[3]) + fabsf(mine[4])) + (fabsf(mine[5]) + fabsf(mine[6]))) + fabsf(mine[7]);
            const bool bad = pass == 0 && __any(!(asum <= 1e30f));
            st_c((u32x4){pk2(mine[0] * rl, mine[1] * rl), pk2(mine[2] * rl, mine[3] * rl), pk2(mine[4] * rl, mine[5] * rl), pk2(mine[6] * rl, mine[7] * rl)},
                 rw, l16, (bad || done) ? OOB : odst);
#pragma unroll
            for (int k = 0; k < NW1; ++k) PIN(a1[k]);
            __builtin_amdgcn_s_setprio(0); SBAR();
            cs1 = 0.f; cs2 = 0.f;
#pragma unroll
            for (int k = 0; k < NW1; ++k) { cs1 = dot2(a1[k][0], ONES, cs1); cs2 = dot2(a1[k][1], ONES, cs2); cs1 = dot2(a1[k][2], ONES, cs1); cs2 = dot2(a1[k][3], ONES, cs2); }
            PIN(cs1); PIN(cs2); SBAR();
            asm volatile("s_waitcnt vmcnt(1)" : "+v"(fpv) :: "memory");
            if (!flag_ok(fpv, ep)) wait_flags<12>(rw, fpo, ep);
            __builtin_amdgcn_s_sleep(4);
            fetch();
            done = !bad;
            if (bad) ((volatile LAS unsigned*)(lds + L_CNT))[6] = 1u;
            BARRIER();
            if (__builtin_amdgcn_readfirstlane(((volatile LAS unsigned*)(lds + L_CNT))[6]) == 0u) break;
            waitv<0>();
            po = ph = pg = ps = OOB;
        }
    }
    if (jj >= 28) {
        if (b == 255 && w == 0) {
#pragma unroll 1
            for (int n = 0; n < 4; ++n) wait_flags(rw, WS_FR + (n * 64 + lane) * 8, ep);
            if (lane == 0) st_flag(rw, WS_EPOCH, ep);
        }
        return;
    }

    unsigned bias;
    u32x4 a2[28];
    const int w2s = WS_W2 + (2 * c3 * 112 + 7 * kw) * 1024;
#define W2O(q) ((((q) & 1) * 112 + ((q) >= 14 ? 56 - 7 : 0) + ((q) >> 1)) * 1024)
    {
        const int nt2 = w & 1, rt = (w >> 1) & 1;
        f32x16 acc;
        u32x4 xr[4];
        auto ldw = [&](int k) {
            if (k < 4) a1[24 + k] = ld_c(rw, l16 + k * 1024, w1s + 6 * 4096);
            else a2[k - 4] = ld_c(rw, l16, w2s + W2O(k - 4));
        };
        int hdst = WS_H + ((r3 * 2 + rt) * 28 + 4 * c3 + 2 * nt2) * 1024, sdst = WS_ST + ((r3 * 2 + rt) * 14 + 2 * c3 + nt2) * 256;
        asm volatile("" : "+s"(hdst), "+s"(sdst));
        bias = *(const LAS u16*)(lds + L_B1 + ((w >> 1) * 64 + 2 * i + (w & 1)) * 2);
#pragma unroll
        for (int g = 0; g < 4; ++g) xr[g] = lds128(lds + L_HS + (rt * 32 + i) * 128 + (((2 * g + h) ^ ((i >> 1) & 7)) << 4));
        asm volatile("" ::: "memory"); SBAR();
#pragma unroll
        for (int k = 0; k < 4; ++k) ldw(k);
        SBAR();
#pragma unroll
        for (int g = 0; g < 4; ++g)
#pragma unroll
            for (int e = 0; e < 4; ++e) acc[4 * g + e] = __uint_as_float(__builtin_amdgcn_perm(xr[g][e], 0u, nt2 ? 0x07060c0cu : 0x05040c0cu));
        PIN(acc); PIN(bias);
        waitv<4>();
#pragma unroll 1
        for (int tries = 0; tries < TRIES; ++tries) {
            unsigned m = 0;
#pragma unroll
            for (int p = 0; p < 8; ++p) m = pmax(m, lds128(lds + 65536 + w * 8192 + p * 1024 + l16));
            if (!poisoned(m)) break;
            fetch();
            asm volatile("s_waitcnt vmcnt(0)" ::: "memory");
        }
        BAR();
        if (w >= 4) {
            const LAS char* pa = lds + nt2 * 32768 + l16; const LAS char* pb = lds + 65536 + rt * 32768 + l16;
            u32x4 fa[4], fb[4];
#pragma unroll
            for (int k = 0; k < 4; ++k) { fa[k] = lds128(pa + k * 1024); fb[k] = lds128(pb + k * 1024); }
            SBAR();
#pragma unroll
            for (int k = 0; k < 32; ++k) {
                acc = mfma(fa[k & 3], fb[k & 3], acc); SBAR();
                if (k < 28) { fa[k & 3] = lds128(pa + (k + 4) * 1024); fb[k & 3] = lds128(pb + (k + 4) * 1024); SBAR(); }
                if (k % 4 == 0 && k / 4 < 6) { ldw(4 + k / 4); SBAR(); }
            }
            u32x4 p0 = pack8(acc, 0), p1 = pack8(acc, 1);
            st_c(p0, rw, l16, hdst); st_c(p1, rw, l16 + 1024, hdst);
            SBAR(); PIN(p0); PIN(p1);
            float s = 0.f, q = 0.f, s2 = 0.f, q2 = 0.f;
#pragma unroll
            for (int e = 0; e < 4; ++e) { s = dot2(p0[e], ONES, s); q = dot2(p0[e], p0[e], q); s2 = dot2(p1[e], ONES, s2); q2 = dot2(p1[e], p1[e], q2); }
            s = sum32(s + s2); q = sum32(q + q2);
            __builtin_amdgcn_raw_buffer_store_b32(__float_as_uint(h ? q : s), rw, lane * 4, sdst, 16);
            BAR();
            *(LAS u32x4*)(lds + L_HS + tt * 2048 + l16) = p0; *(LAS u32x4*)(lds + L_HS + tt * 2048 + 1024 + l16) = p1;
        } else {
#pragma unroll
            for (int k = 4; k < 22; ++k) ldw(k);
            BAR(); __builtin_amdgcn_s_sleep(4);
        }
#pragma unroll
        for (int k = 24; k < 28; ++k) { cs1 = dot2(a1[k][0], ONES, cs1); cs2 = dot2(a1[k][1], ONES, cs2); cs1 = dot2(a1[k][2], ONES, cs1); cs2 = dot2(a1[k][3], ONES, cs2); }
        cs1 = sum32(cs1 + cs2);
    }
    u32x4 bf[14];
    int ha[4], hb[4];
#pragma unroll
    for (int q = 0; q < 4; ++q) { ha[q] = WS_HID + (r3 * 224 + 7 * kw + (q >> 1) * 56 + (q & 1) * 4) * 1024; hb[q] = ha[q] + 112 * 1024; }
#define LDH(k, so) asm volatile("buffer_load_dwordx4 %0, %1, %2, %3 offen offset:%4 sc1" : "=v"(bf[k]) : "v"(l16), "s"(rw), "s"(so[((k) / 7) * 2 + (((k) % 7) >> 2)]), "n"((((k) % 7) & 3) * 1024) : "memory");
#define FOR7(M, g, so) M(7 * g, so) M(7 * g + 1, so) M(7 * g + 2, so) M(7 * g + 3, so) M(7 * g + 4, so) M(7 * g + 5, so) M(7 * g + 6, so)
    {
        unsigned ch, cl; split2(cs1, ch, cl);
        u32x4 ae = {h ? 0u : (ch | (cl << 16)), h ? 0u : (ch | (cl << 16)), h ? 0u : (bias | (bias << 16)), 0u};
        PIN(ae);
#pragma unroll 1
        for (int tries = 0; tries < TRIES; ++tries) {
            dma_c4(rw, l16, WS_H + r3 * 57344 + w * 7168, lbase + w * 7168);
#pragma unroll
            for (int p = 4; p < 7; ++p) dma_c(rw, l16, WS_H + r3 * 57344 + w * 7168 + p * 1024, lbase + w * 7168 + p * 1024);
            dma_c(rw, l16, w < 7 ? WS_ST + r3 * 7168 + w * 1024 : OOB, lbase + 57344 + w * 1024);
            waitv<0>();
            unsigned m = pmax(0u, lds128(lds + 57344 + w * 1024 + l16));
#pragma unroll
            for (int p = 0; p < 7; ++p) m = pmax(m, lds128(lds + w * 7168 + p * 1024 + l16));
            if (!poisoned(m)) break;
        }
        BARRIER();
        auto park = [&](int n) {
            dma_c4(rw, l16, w2s + W2O(18 + n), lbase + 65536 + w * 10240 + n * 5120); dma_c(rw, l16, w2s + W2O(26 + n), lbase + 65536 + w * 10240 + n * 5120 + 4096);
        };
        if (w >= 4) {
#pragma unroll
            for (int q = 6; q < 18; ++q) a2[q] = ld_c(rw, l16, w2s + W2O(q));
            park(0); park(1);
        }
        u32x4 fb[4];
#pragma unroll
        for (int k = 0; k < 4; ++k) fb[k] = lds128(lds + k * 1024 + l16);
#pragma unroll
        for (int rt = 0; rt < 2; ++rt) {
            f32x16 acc = zero;
            const LAS float* sp = (const LAS float*)(lds + 57344) + rt * 14 * 64 + lane;
            float st = 0.f, sv[14];
            u32x4 be; float g1, g2, sd, mean, var, rs;
            if (rt) __builtin_amdgcn_s_setprio(2); else __builtin_amdgcn_s_setprio(3);
            SBAR();
#pragma unroll
            for (int k = 0; k < 28; ++k) {
                acc = mfma(a1[k], fb[k & 3], acc); SBAR();
                if (rt * 28 + k + 4 < 56) fb[k & 3] = lds128(lds + (rt * 28 + k + 4) * 1024 + l16);
                if (k < 7) { sv[2 * k] = sp[2 * k * 64]; sv[2 * k + 1] = sp[(2 * k + 1) * 64]; }
                if (k >= 4 && k < 11) { st += sv[2 * (k - 4)] + sv[2 * (k - 4) + 1]; PIN(st); }
                if (k == 12) { both32(st, mean, var); mean *= 1.f / D_; PIN(mean); PIN(var); }
                if (k == 13) { var = fmaxf(var * (1.f / D_) - mean * mean, 0.f); rs = __builtin_amdgcn_rsqf(var + EPS); PIN(var); PIN(rs); }
                if (k == 14) { var = __builtin_amdgcn_rsqf(var * rs * rs + EPS); PIN(var); }
                if (k == 15) { rs *= var; sd = __builtin_amdgcn_rcpf(rs); g1 = -2.3022081981f * rs; PIN(rs); PIN(sd); PIN(g1); }
                if (k == 16) { g2 = -0.1029432397f * rs * rs * rs; be[0] = pk2(-mean, -mean); PIN(g2); PIN(be[0]); }
                if (k == 17) { const float r_ = -mean - bflo(be[0]); be[1] = pk2(r_, r_); be[0] = h ? 0u : be[0]; PIN(be[0]); PIN(be[1]); }
                if (k == 18) { be[2] = pk2(sd, sd); be[1] = h ? 0u : be[1]; PIN(be[1]); PIN(be[2]); }
                if (k == 19) { be[2] = pk2(sd, sd - bflo(be[2])); be[2] = h ? 0u : be[2]; be[3] = 0u; PIN(be); }
                if (k == 21) acc = mfma(ae, be, acc);
                if (rt == 1 && k == 13) waitv<2>();
                if (rt == 1 && k >= 14 && k < 24) a2[k + 4] = lds128(lds + 65536 + w * 10240 + (k & 1) * 5120 + ((k - 14) >> 1) * 1024 + l16);
                SBAR();
            }
            __builtin_amdgcn_s_setprio(1);
            if (rt) {
#pragma unroll
                for (int q = 0; q < 28; ++q) PIN(a2[q]);
                asm volatile("s_nop 4" : "+s"(ha[0]), "+s"(ha[1]), "+s"(ha[2]), "+s"(ha[3]), "+s"(hb[0]), "+s"(hb[1]), "+s"(hb[2]), "+s"(hb[3]));
            }
            SBAR();
#pragma unroll
            for (int hf = 0; hf < 2; ++hf) {
                const f32x8 a = hf ? __builtin_shufflevector(acc, acc, 8, 9, 10, 11, 12, 13, 14, 15) : __builtin_shufflevector(acc, acc, 0, 1, 2, 3, 4, 5, 6, 7);
                f32x8 u;
#define ST8(EXPR) _Pragma("unroll") for (int j = 0; j < 8; ++j) { float t_ = (EXPR); asm volatile("" : "+v"(t_)); u[j] = t_; } SBAR();
#define LA(n) if (rt) { if (hf) { LDH(7 + n, ha) } else { LDH(n, ha) } SBAR(); } else if (n == 4 && w < 4) { park(hf); SBAR(); }
                if (rt && !hf) spin_lds((volatile LAS unsigned*)(lds + L_CNT) + 13, 8);
                LA(0) ST8(a[j] * a[j]) LA(1) ST8(__builtin_fmaf(u[j], g2, g1)) LA(2) ST8(u[j] * a[j]) LA(3) ST8(__builtin_amdgcn_exp2f(u[j]))
                LA(4) ST8(__builtin_fmaf(u[j], sd, sd)) LA(5) ST8(__builtin_amdgcn_rcpf(u[j])) LA(6) ST8(u[j] * a[j])
#pragma unroll
                for (int j = 0; j < 8; ++j) acc[8 * hf + j] = u[j];
            }
            const int dst = WS_HID + ((r3 * 2 + rt) * 112 + kh * 56 + 8 * c3 + 2 * tt) * 1024;
            u32x4 g0 = pack8(acc, 0), g8 = pack8(acc, 1);
            st_c(g0, rw, l16, dst); st_c(g8, rw, l16 + 1024, dst);
            if (!rt) asm volatile("s_mov_b64 exec, 1\n\tds_add_u32 %0, %1\n\ts_mov_b64 exec, -1" :: "v"(lbase + L_CNT + 52), "v"(1u) : "memory");
            SBAR();
        }
    }
    {
        volatile LAS unsigned* cnt = (volatile LAS unsigned*)(lds + L_CNT);
        __builtin_amdgcn_s_setprio(0);
        auto put = [&](const f32x16& acc0, const f32x16& acc1, int rt) {
#pragma unroll
            for (int p = 0; p < 8; ++p) {
                LAS f32x2* red = (LAS f32x2*)(lds + (rt ? 0 : 65536) + w * 8192 + p * 1024 + h * 512 + ((i + 4 * (p & 3) + 2 * h) & 31) * 16);
                red[0] = (f32x2){acc0[2 * p], acc0[2 * p + 1]}; red[1] = (f32x2){acc1[2 * p], acc1[2 * p + 1]};
            }
            SBAR();
        };
        const rsrc_t ro = __builtin_amdgcn_make_buffer_rsrc(out, 0, 2048 * 896, 0x00020000);
        struct Fin { f32x4 base; const LAS f32x4* red; int vo, so; };
        auto prep = [&](int rt, int rg) {
            const int row = 4 * rg + (lane >> 4), cq = lane & 15, hh = (cq >> 1) & 1, p = (cq & 1) + 2 * (cq >> 2);
            const u32x2 bb = *(const LAS u32x2*)(lds + L_B2 + cq * 8);
            const LAS char* hp = lds + L_HS + rt * 4096 + (p >> 2) * 1024 + (hh * 32 + row) * 16 + (p & 3) * 4;
            const unsigned h0 = *(const LAS unsigned*)hp, h1 = *(const LAS unsigned*)(hp + 2048);
            Fin f;
            f.base = (f32x4){bflo(bb[0]) + bflo(h0), bflo(bb[1]) + bfhi(h0), bfhi(bb[0]) + bflo(h1), bfhi(bb[1]) + bfhi(h1)};
            f.red = (const LAS f32x4*)(lds + (rt ? 0 : 65536) + p * 1024 + hh * 512 + ((row + 4 * (p & 3) + 2 * hh) & 31) * 16);
            f.vo = row * 896 + cq * 8; f.so = (r3 * 64 + rt * 32) * 896 + c3 * 128;
            PIN(f.base); PIN(f.red); PIN(f.vo); asm volatile("" : "+s"(f.so));
            return f;
        };
        auto fin = [&](const Fin& f) {
            f32x4 t[8];
#pragma unroll
            for (int q = 0; q < 8; ++q) t[q] = f.red[q * 512];
            SBAR();
            const f32x4 r = (((f.base + t[0]) + t[1]) + (t[2] + t[3])) + ((t[4] + t[5]) + (t[6] + t[7]));
            __builtin_amdgcn_raw_buffer_store_b64((u32x2){pk2(r[0], r[2]), pk2(r[1], r[3])}, ro, f.vo, f.so, 16);
        };
        asm volatile("s_mov_b64 exec, 1\n\tds_add_u32 %0, %1\n\ts_mov_b64 exec, -1" :: "v"(lbase + L_CNT + 40), "v"(1u) : "memory");
#define WAIT7(g, n) asm volatile("s_waitcnt vmcnt(" #n ")" : "+v"(bf[7 * g]), "+v"(bf[7 * g + 1]), "+v"(bf[7 * g + 2]), "+v"(bf[7 * g + 3]), "+v"(bf[7 * g + 4]), "+v"(bf[7 * g + 5]), "+v"(bf[7 * g + 6]) :: "memory")
#define CHECK7(g, n, so) do { WAIT7(g, n); _Pragma("unroll 1") for (int tries = 0; tries < TRIES; ++tries) { \
            const unsigned m0 = pmax(pmax(pmax(pmax(0u, bf[7 * g]), bf[7 * g + 2]), bf[7 * g + 4]), bf[7 * g + 6]), m1 = pmax(pmax(pmax(0u, bf[7 * g + 1]), bf[7 * g + 3]), bf[7 * g + 5]); \
            if (!poisoned(max(m0, m1))) break; \
            FOR7(LDH, g, so) WAIT7(g, 0); } } while (0)
#define MM(k, so) acc0 = mfma(a2[2 * (k)], bf[k], acc0); acc1 = mfma(a2[2 * (k) + 1], bf[k], acc1); SBAR();
#define MML(k, so) MM(k, so) LDH((k) - 7, so) SBAR();
        f32x16 acc0 = zero, acc1 = zero;
        CHECK7(0, 9, ha);
        FOR7(MM, 0, hb)
        CHECK7(1, 2, ha);
        FOR7(MML, 1, hb)
        spin_lds(cnt + 10, 8);
        if (w < 4) { put(acc0, acc1, 0); __builtin_amdgcn_s_sleep(4); }
        FOR7(LDH, 1, hb)
        if (w >= 4) put(acc0, acc1, 0);
        asm volatile("s_waitcnt lgkmcnt(0)\n\ts_mov_b64 exec, 1\n\tds_add_u32 %0, %1\n\ts_mov_b64 exec, -1" :: "v"(lbase + L_CNT + 44), "v"(1u) : "memory");
        acc0 = zero; acc1 = zero;
        const Fin fl = prep(1, w);
        CHECK7(0, 7, hb);
        FOR7(MM, 0, hb)
        CHECK7(1, 0, hb);
        FOR7(MM, 1, hb)
        put(acc0, acc1, 1);
        if (w < 4) {
            spin_lds(cnt + 11, 8);
            fin(prep(0, w)); fin(prep(0, w + 4));
        }
        BARRIER();
        fin(fl);
    }
    asm volatile("88:");
}

extern "C" void kernel_launch(void* const* d_in, const int* in_sizes, int n_in, void* d_out, int out_size, void* d_ws, size_t ws_size, hipStream_t stream) {
    (void)hipFuncSetAttribute((const void*)fused_block, hipFuncAttributeMaxDynamicSharedMemorySize, LDS_BYTES);
    fused_block<<<256, 512, LDS_BYTES, stream>>>((const u16*)d_in[0], (const u16*)d_in[1], (const u16*)d_in[2], (const u16*)d_in[4], (const u16*)d_in[5],
                                                 (const u16*)d_in[6], (const u16*)d_in[7], (u16*)d_out, (char*)d_ws);
}
```
